```python
import torch

def _softmax(x, axis=-1):
    return torch.softmax(x, dim=axis)
def _gelu(x):
    return torch.nn.functional.gelu(x, approximate="tanh")
def _silu(x):
    return torch.nn.functional.silu(x)
def _sigmoid(x):
    return torch.sigmoid(x)
def _layernorm(x, axis=-1, eps=1e-5):
    m = x.mean(dim=axis, keepdim=True)
    v = x.var(dim=axis, keepdim=True, unbiased=False)
    return (x - m) / torch.sqrt(v + eps)
def _scatter_add(tgt, idx, src, axis=0):
    return tgt.clone().index_add_(axis, idx, src)
def _segment_sum(src, seg, n):
    out = torch.zeros((n,) + src.shape[1:], dtype=src.dtype)
    return out.index_add_(0, seg, src)
def _pad(x, pw):
    flat = tuple(v for pair in reversed(pw) for v in pair)
    return torch.nn.functional.pad(x, flat)

kernel_name = 'standalone__detection_seg_mask2former__vision'

def setup_inputs():

    _w = (lambda a: (lambda t: t.to(torch.bfloat16) if t.is_floating_point() else t)(torch.as_tensor(a)))
    H = W = 512
    C = 192
    Hc, Wc = H // 2, W // 2
    nh = 4
    Nq = 16
    sc = C ** -0.5
    return {
        "q": _w((lambda *s: torch.randn(*s, dtype=torch.bfloat16) * 0.4)(1, Nq, C)),
        "f_hi": _w((lambda *s: torch.randn(*s, dtype=torch.bfloat16) * 0.4)(1, H, W, C)),
        "f_lo": _w((lambda *s: torch.randn(*s, dtype=torch.bfloat16) * 0.4)(1, Hc, Wc, C)),
        "w_lat": _w((lambda *s: torch.randn(*s, dtype=torch.bfloat16) * 0.4)(C, C) * sc),
        "w_q": _w((lambda *s: torch.randn(*s, dtype=torch.bfloat16) * 3.2)(nh, C, C // nh) * sc),
        "w_k": _w((lambda *s: torch.randn(*s, dtype=torch.bfloat16) * 3.2)(nh, C, C // nh) * sc),
        "w_v": _w((lambda *s: torch.randn(*s, dtype=torch.bfloat16) * 3.2)(nh, C, C // nh) * sc),
        "w_o": _w((lambda *s: torch.randn(*s, dtype=torch.bfloat16) * 3.2)(C, C) * sc),
        "w_m": _w((lambda *s: torch.randn(*s, dtype=torch.bfloat16) * 3.2)(C, C) * sc),
    }

def reference(q, f_hi, f_lo, w_lat, w_q, w_k, w_v, w_o, w_m):
    B, Nq, C = q.shape
    _, H, W, _ = f_hi.shape
    nh = w_q.shape[0]
    dh = C // nh
    up = (lambda x, n, axis: torch.repeat_interleave(x, n, dim=axis))((lambda x, n, axis: torch.repeat_interleave(x, n, dim=axis))(f_lo, 2, axis=1), 2, axis=2)
    feat = f_hi + up @ w_lat
    feat = _layernorm(feat, axis=-1)
    kv = feat.reshape(B, H * W, C)
    m_embed = q @ w_m
    mask_bias = torch.einsum('bqc,bkc->bqk', m_embed, kv) * (C ** -0.5)
    Q = torch.einsum('bqc,hcd->bhqd', q, w_q)
    K = torch.einsum('bkc,hcd->bhkd', kv, w_k)
    V = torch.einsum('bkc,hcd->bhkd', kv, w_v)
    logits = torch.einsum('bhqd,bhkd->bhqk', Q, K) * (dh ** -0.5)
    logits = logits + mask_bias[:, None, :, :]
    attn = _softmax(logits, axis=-1)
    ctx = torch.einsum('bhqk,bhkd->bqhd', attn, V).reshape(B, Nq, C)
    return q + ctx @ w_o
```

```cpp
#include <hip/hip_runtime.h>
#include <hip/hip_bf16.h>
typedef unsigned short u16;
__device__ __forceinline__ float b2f(u16 v) { return __uint_as_float((unsigned)v << 16); }
__device__ __forceinline__ u16 f2b(float f) { unsigned u = __float_as_uint(f); u += 0x7fff + ((u >> 16) & 1); return (u16)(u >> 16); }
__global__ void n_prep(const u16* q, const u16* wq, const u16* wk, const u16* wm, float* A) {
  __shared__ float Q[48];
  int hq = blockIdx.x, h = hq >> 4, qi = hq & 15, t = threadIdx.x;
  if (t < 48) { float a = 0; for (int c = 0; c < 192; ++c) a += b2f(q[qi * 192 + c]) * b2f(wq[(h * 192 + c) * 48 + t]); Q[t] = a; }
  __syncthreads();
  float m = 0; for (int c = 0; c < 192; ++c) m += b2f(q[qi * 192 + c]) * b2f(wm[c * 192 + t]);
  float a = 0; for (int d = 0; d < 48; ++d) a += Q[d] * b2f(wk[(h * 192 + t) * 48 + d]);
  A[hq * 192 + t] = a * rsqrtf(48.f) + m * rsqrtf(192.f);
}
__global__ __launch_bounds__(192) void n_main(const u16* fhi, const u16* flo, const u16* wlat, const float* A, float* part) {
  __shared__ float lat[16][192];
  __shared__ float x[64][193];
  __shared__ float p[64][65];
  __shared__ float Ms[64], ls[64];
  int b = blockIdx.x, row = b >> 4, seg = b & 15, t = threadIdx.x;
  for (int i = 0; i < 16; ++i) { const u16* fl = flo + ((size_t)row * 256 + seg * 16 + i) * 192; float a = 0; for (int c = 0; c < 192; ++c) a += b2f(fl[c]) * b2f(wlat[c * 192 + t]); lat[i][t] = a; }
  __syncthreads();
  for (int i = 0; i < 64; ++i) { int dy = i >> 5, xx = i & 31; x[i][t] = b2f(fhi[((size_t)(2 * row + dy) * 512 + seg * 32 + xx) * 192 + t]) + lat[xx >> 1][t]; }
  __syncthreads();
  if (t < 64) { float s = 0; for (int c = 0; c < 192; ++c) s += x[t][c]; s /= 192.f; float v = 0; for (int c = 0; c < 192; ++c) { float d = x[t][c] - s; v += d * d; } v /= 192.f; float r = 1.f / sqrtf(v + 1e-5f); for (int c = 0; c < 192; ++c) x[t][c] = (x[t][c] - s) * r; }
  __syncthreads();
  for (int e = t; e < 4096; e += 192) { int hq = e >> 6, px = e & 63; float a = 0; for (int c = 0; c < 192; ++c) a += A[hq * 192 + c] * x[px][c]; p[hq][px] = a; }
  __syncthreads();
  if (t < 64) { float m = -1e30f; for (int i = 0; i < 64; ++i) m = fmaxf(m, p[t][i]); float l = 0; for (int i = 0; i < 64; ++i) { float e = expf(p[t][i] - m); p[t][i] = e; l += e; } Ms[t] = m; ls[t] = l; }
  __syncthreads();
  float* o = part + (size_t)b * 64 * 194;
  for (int hq = 0; hq < 64; ++hq) { float a = 0; for (int i = 0; i < 64; ++i) a += p[hq][i] * x[i][t]; o[hq * 194 + t] = a; }
  if (t < 64) { o[t * 194 + 192] = Ms[t]; o[t * 194 + 193] = ls[t]; }
}
__global__ __launch_bounds__(192) void n_red(const float* part, float* U) {
  int hq = blockIdx.x, t = threadIdx.x; float m = -1e30f;
  for (int b = 0; b < 4096; ++b) m = fmaxf(m, part[((size_t)b * 64 + hq) * 194 + 192]);
  double l = 0, u = 0;
  for (int b = 0; b < 4096; ++b) { const float* o = part + ((size_t)b * 64 + hq) * 194; float s = expf(o[192] - m); l += s * o[193]; u += s * o[t]; }
  U[hq * 192 + t] = (float)(u / l);
}
__global__ __launch_bounds__(192) void n_fin(const float* U, const u16* q, const u16* wv, const u16* wo, u16* out) {
  __shared__ float ctx[192];
  int qi = blockIdx.x, t = threadIdx.x, h = t / 48, d = t % 48;
  float a = 0; for (int c = 0; c < 192; ++c) a += U[(h * 16 + qi) * 192 + c] * b2f(wv[(h * 192 + c) * 48 + d]);
  ctx[t] = a; __syncthreads();
  float o = 0; for (int c = 0; c < 192; ++c) o += ctx[c] * b2f(wo[c * 192 + t]);
  out[qi * 192 + t] = f2b(b2f(q[qi * 192 + t]) + o);
}
#define NAIVE_NAME kernel_launch
extern "C" void NAIVE_NAME(void* const* d_in, const int* in_sizes, int n_in, void* d_out, int out_size, void* d_ws, size_t ws_size, hipStream_t stream) {
  const u16* q = (const u16*)d_in[0]; const u16* fhi = (const u16*)d_in[1]; const u16* flo = (const u16*)d_in[2]; const u16* wlat = (const u16*)d_in[3];
  const u16* wq = (const u16*)d_in[4]; const u16* wk = (const u16*)d_in[5]; const u16* wv = (const u16*)d_in[6]; const u16* wo = (const u16*)d_in[7]; const u16* wm = (const u16*)d_in[8];
  float* A = (float*)d_ws; float* U = A + 64 * 192; float* part = U + 64 * 192;
  n_prep<<<64, 192, 0, stream>>>(q, wq, wk, wm, A);
  n_main<<<4096, 192, 0, stream>>>(fhi, flo, wlat, A, part);
  n_red<<<64, 192, 0, stream>>>(part, U);
  n_fin<<<16, 192, 0, stream>>>(U, q, wv, wo, (u16*)d_out);
}
```

```cpp
#include <hip/hip_runtime.h>
#include <hip/hip_bf16.h>

typedef unsigned short u16;
typedef unsigned v4u __attribute__((ext_vector_type(4)));
typedef unsigned v2u __attribute__((ext_vector_type(2)));
typedef float v4f __attribute__((ext_vector_type(4)));
typedef short v4s __attribute__((ext_vector_type(4)));
typedef __bf16 v8bf __attribute__((ext_vector_type(8)));
typedef __bf16 v2bf __attribute__((ext_vector_type(2)));
#define LAS __attribute__((address_space(3)))
#define GAS __attribute__((address_space(1)))
#define DEV __device__ __forceinline__

constexpr unsigned L_F = 0;
constexpr unsigned L_R = 98304;
constexpr unsigned L_P = 135168;
constexpr unsigned L_POOL = 151552;
constexpr unsigned L_SIG = 159744;
constexpr unsigned L_MW = 160256;
constexpr unsigned L_FLAG = 161280;
constexpr unsigned L_TOTAL = 161344;
constexpr float HEADROOM = 40.f;

DEV float b2f(u16 v) { return __uint_as_float((unsigned)v << 16); }
DEV unsigned pk2(float a, float b) { v2bf r; r[0] = (__bf16)a; r[1] = (__bf16)b; return __builtin_bit_cast(unsigned, r); }
DEV v4f mma(v4u a, v4u b, v4f c) { return __builtin_amdgcn_mfma_f32_16x16x32_bf16(__builtin_bit_cast(v8bf, a), __builtin_bit_cast(v8bf, b), c, 0, 0, 0); }
DEV v4u pack8(v4f a, v4f b) { return (v4u){pk2(a[0], a[1]), pk2(a[2], a[3]), pk2(b[0], b[1]), pk2(b[2], b[3])}; }

extern __shared__ __attribute__((aligned(16))) char g_lds[];
DEV LAS char* lp(unsigned off) { return (LAS char*)g_lds + off; }
DEV v4u ld128(unsigned off) { return *(LAS v4u*)lp(off); }
DEV v4f ld128f(unsigned off) { return *(LAS v4f*)lp(off); }
DEV void st128(unsigned off, v4u v) { *(LAS v4u*)lp(off) = v; }
DEV v2u ldtr(unsigned off) { return __builtin_bit_cast(v2u, __builtin_amdgcn_ds_read_tr16_b64_v4i16((LAS v4s*)lp(off))); }
DEV v4u ldtr2(unsigned o0, unsigned o1) { v2u a = ldtr(o0), b = ldtr(o1); return (v4u){a[0], a[1], b[0], b[1]}; }

DEV void dma16(unsigned dst, unsigned voff, const char* sbase) {
  asm volatile("s_mov_b32 m0, %0\n\ts_nop 0\n\tglobal_load_lds_dwordx4 %1, %2 nt" ::"s"(dst), "v"(voff), "s"(sbase) : "memory");
}
DEV void dma16c(unsigned dst, unsigned voff, const char* sbase) {
  asm volatile("s_mov_b32 m0, %0\n\ts_nop 0\n\tglobal_load_lds_dwordx4 %1, %2" ::"s"(dst), "v"(voff), "s"(sbase) : "memory");
}
template <int N> DEV void bar_vm() { asm volatile("s_waitcnt vmcnt(%0) lgkmcnt(0)\n\ts_barrier" ::"n"(N) : "memory"); }
DEV void bar() { asm volatile("s_waitcnt lgkmcnt(0)\n\ts_barrier" ::: "memory"); }

DEV int lane_now() { int l; asm volatile("v_mbcnt_lo_u32_b32 %0, -1, 0\n\tv_mbcnt_hi_u32_b32 %0, -1, %0" : "=v"(l)); return l; }
DEV unsigned fsw(unsigned row) { unsigned u = (row >> 1) & 7; return (u & 4) | ((u & 1) << 1) | ((u >> 1) & 1); }

DEV float sum_groups(float v) {
  unsigned x = __float_as_uint(v);
  auto a = __builtin_amdgcn_permlane32_swap(x, x, false, false);
  float s = __uint_as_float(a[0]) + __uint_as_float(a[1]);
  x = __float_as_uint(s);
  auto b = __builtin_amdgcn_permlane16_swap(x, x, false, false);
  return __uint_as_float(b[0]) + __uint_as_float(b[1]);
}
DEV float diag16(v4f d, int dsel) {
  float v = dsel == 0 ? d[0] : 0.f;
  v = dsel == 1 ? d[1] : v;
  v = dsel == 2 ? d[2] : v;
  v = dsel == 3 ? d[3] : v;
  return sum_groups(v);
}
DEV float rowmax16(float v) {
  v = fmaxf(v, __uint_as_float(__builtin_amdgcn_update_dpp(0, __float_as_uint(v), 0x128, 0xf, 0xf, true)));
  v = fmaxf(v, __uint_as_float(__builtin_amdgcn_update_dpp(0, __float_as_uint(v), 0x124, 0xf, 0xf, true)));
  v = fmaxf(v, __uint_as_float(__builtin_amdgcn_update_dpp(0, __float_as_uint(v), 0x122, 0xf, 0xf, true)));
  v = fmaxf(v, __uint_as_float(__builtin_amdgcn_update_dpp(0, __float_as_uint(v), 0x121, 0xf, 0xf, true)));
  return v;
}

__global__ __launch_bounds__(192) void m2f_prep(const u16* __restrict__ q, const u16* __restrict__ wq, const u16* __restrict__ wk,
                                                const u16* __restrict__ wm, u16* __restrict__ Af) {
  __shared__ float Q[48];
  __shared__ float red[3];
  const int hq = blockIdx.x, h = hq >> 4, qi = hq & 15, t = threadIdx.x;
  if (t < 48) { float a = 0; for (int c = 0; c < 192; ++c) a += b2f(q[qi * 192 + c]) * b2f(wq[(h * 192 + c) * 48 + t]); Q[t] = a; }
  float m = 0;
  for (int c = 0; c < 192; ++c) m += b2f(q[qi * 192 + c]) * b2f(wm[c * 192 + t]);
  __syncthreads();
  float a = 0;
  for (int d = 0; d < 48; ++d) a += Q[d] * b2f(wk[(h * 192 + t) * 48 + d]);
  a = (a * 0.14433756729740643f + m * 0.07216878364870322f) * 1.4426950408889634f;
  float s = a;
  for (int o = 32; o >= 1; o >>= 1) s += __shfl_xor(s, o);
  if ((t & 63) == 0) red[t >> 6] = s;
  __syncthreads();
  a -= (red[0] + red[1] + red[2]) * (1.f / 192.f);
  const int qt = 2 * (hq >> 5) + ((hq >> 2) & 1), i = 4 * ((hq >> 3) & 3) + (hq & 3);
  const int c8 = t >> 3, ks = 2 * (c8 >> 3) + (c8 & 1), G = (c8 >> 1) & 3;
  Af[((qt * 6 + ks) * 64 + G * 16 + i) * 8 + (t & 7)] = __builtin_bit_cast(u16, (__bf16)a);
}

struct Sel { v4u s0, s1; };
DEV Sel make_sel(int G, int i) {
  Sel s;
#pragma unroll
  for (int j = 0; j < 4; ++j) {
    int k0 = 8 * G + 2 * j;
    s.s0[j] = (k0 == i ? 0x3f80u : 0u) | (k0 + 1 == i ? 0x3f800000u : 0u);
    s.s1[j] = (k0 == i + 16 ? 0x3f80u : 0u) | (k0 + 1 == i + 16 ? 0x3f800000u : 0u);
  }
  return s;
}
DEV v4u packx(v4f a, v4f b) {
  return (v4u){__builtin_amdgcn_perm(__float_as_uint(a[1]), __float_as_uint(a[0]), 0x07060302u), __builtin_amdgcn_perm(__float_as_uint(a[3]), __float_as_uint(a[2]), 0x07060302u),
               __builtin_amdgcn_perm(__float_as_uint(b[1]), __float_as_uint(b[0]), 0x07060302u), __builtin_amdgcn_perm(__float_as_uint(b[3]), __float_as_uint(b[2]), 0x07060302u)};
}
DEV void transpose_w(v4u (&W)[3][6], const Sel& s) {
  const v4f z = {0.f, 0.f, 0.f, 0.f};
#pragma unroll
  for (int ks = 0; ks < 6; ++ks) {
    v4f a0 = mma(W[0][ks], s.s0, z), a1 = mma(W[1][ks], s.s0, z), b0 = mma(W[0][ks], s.s1, z), b1 = mma(W[1][ks], s.s1, z);
    v4f c0 = mma(W[2][ks], s.s0, z), c1 = mma(W[2][ks], s.s1, z);
    W[0][ks] = packx(a0, a1); W[1][ks] = packx(b0, b1); W[2][ks] = packx(c0, c1);
  }
}

struct PvState {
  v4u W[3][6];
  v4f U[4][3];
  v4f l;
  v4u latA[2][3], latB[2][3];
  float M;
  unsigned aF[3][2], aP[4], aSig, aLo, aT, hoff[6], loff[3];
};

DEV void pv_tile(PvState& s, unsigned slot) {
  const unsigned pb = slot & 1;
#pragma unroll
  for (int k2 = 0; k2 < 2; ++k2) {
    v4u P[4], F[3];
#pragma unroll
    for (int t = 0; t < 4; ++t) P[t] = ldtr2(s.aP[t] + pb * 8192 + k2 * 2048, s.aP[t ^ 2] + pb * 8192 + k2 * 2048 + 512);
#pragma unroll
    for (int ct = 0; ct < 3; ++ct) F[ct] = ldtr2(s.aF[ct][0] + slot * 6144 + k2 * 24576, s.aF[ct][1] + slot * 6144 + k2 * 24576);
    v4u sg = ld128(s.aSig + pb * 256 + k2 * 32);
#pragma unroll
    for (int ct = 0; ct < 3; ++ct)
#pragma unroll
      for (int t = 0; t < 4; ++t) s.U[t][ct] = mma(F[ct], P[t], s.U[t][ct]);
    s.l = mma(sg, P[0], s.l);
  }
}
DEV void pv_lo(PvState& s, v4u (&lat)[2][3]) {
#pragma unroll
  for (int k2 = 0; k2 < 2; ++k2) {
    v4u P[4];
#pragma unroll
    for (int t = 0; t < 4; ++t) P[t] = ldtr2(s.aP[t] + 16384 + k2 * 2048, s.aP[t ^ 2] + 16384 + k2 * 2048 + 512);
#pragma unroll
    for (int ct = 0; ct < 3; ++ct)
#pragma unroll
      for (int t = 0; t < 4; ++t) s.U[t][ct] = mma(lat[k2][ct], P[t], s.U[t][ct]);
  }
}
DEV void lat_pair(PvState& s, v4u (&out)[3], unsigned src, unsigned dst) {
  v4f acc[2][3];
#pragma unroll
  for (int a = 0; a < 2; ++a)
#pragma unroll
    for (int ct = 0; ct < 3; ++ct) acc[a][ct] = (v4f){0.f, 0.f, 0.f, 0.f};
  const unsigned a0 = s.aLo + src, a1 = (s.aLo ^ 16) + src;
#pragma unroll
  for (int ks = 0; ks < 6; ++ks)
#pragma unroll
    for (int a = 0; a < 2; ++a) {
      v4u x = ld128(((ks & 1) ? a1 : a0) + a * 6144 + 128 * (ks >> 1));
#pragma unroll
      for (int ct = 0; ct < 3; ++ct) acc[a][ct] = mma(x, s.W[ct][ks], acc[a][ct]);
    }
#pragma unroll
  for (int ct = 0; ct < 3; ++ct) {
    out[ct] = pack8(acc[0][ct], acc[1][ct]);
    st128(((ct & 1) ? (s.aT ^ 128) : s.aT) + dst + ct * 512, out[ct]);
  }
}
DEV void pv_check(PvState& s, int k, int w) {
  const unsigned fl = __builtin_amdgcn_readfirstlane(*(LAS unsigned*)lp(L_FLAG + (k & 1) * 4));
  if (__builtin_expect(fl == (unsigned)(k + 1), 0)) {
    bar();
    const int i = lane_now() & 15;
#pragma unroll
    for (int t = 0; t < 4; ++t) {
      const unsigned o = L_MW + (((t ^ w) * 16 + i) << 2);
      float m = fmaxf(fmaxf(*(LAS float*)lp(o), *(LAS float*)lp(o + 256)), fmaxf(*(LAS float*)lp(o + 512), *(LAS float*)lp(o + 768)));
      const float d = fminf(fmaxf(ceilf(m) + HEADROOM, k > 0 ? 0.f : -1.0e6f), 1.0e6f);
      const int e = -(int)d;
#pragma unroll
      for (int ct = 0; ct < 3; ++ct)
#pragma unroll
        for (int r = 0; r < 4; ++r) s.U[t][ct][r] = __builtin_amdgcn_ldexpf(s.U[t][ct][r], e);
      if (t == 0) {
#pragma unroll
        for (int r = 0; r < 4; ++r) s.l[r] = __builtin_amdgcn_ldexpf(s.l[r], e);
        s.M += d;
      }
    }
    bar();
  }
}
DEV void issue_hi(const PvState& s, const char* hb, int k, int w) {
  const char* tb = hb + ((k >> 1) & 1) * 196608 + (k >> 2) * 49152 + (k & 1) * 384;
  const unsigned dst = L_F + w * 24576 + (k & 3) * 6144;
#pragma unroll
  for (int j = 0; j < 6; ++j) dma16(dst + j * 1024, s.hoff[j], tb);
}
DEV void issue_lo(const PvState& s, const char* lb, int g, int pair, unsigned area, int w) {
  const char* tb = lb + g * 24576 + pair * 6144;
#pragma unroll
  for (int j = 0; j < 3; ++j) dma16(area + w * 3072 + j * 1024, s.loff[j], tb);
}
DEV unsigned area(int n) { return L_R + n * 12288; }

__global__ __launch_bounds__(512, 2) void m2f_main(const char* __restrict__ fhi, const char* __restrict__ flo, const char* __restrict__ wlat,
                                                   const char* __restrict__ wv, const char* __restrict__ Af, float* __restrict__ part) {
  const int wave = __builtin_amdgcn_readfirstlane(threadIdx.x >> 6), w = wave & 3;
  const char* hb = fhi + (size_t)blockIdx.x * 393216u;
  const char* lb = flo + (size_t)blockIdx.x * 98304u;
  const v4f z = {0.f, 0.f, 0.f, 0.f};
  const v4u ones = {0x3f803f80u, 0x3f803f80u, 0x3f803f80u, 0x3f803f80u};

  if (wave >= 4) {
    PvState s;
    const int lane = lane_now(), G = lane >> 4, i = lane & 15;
#pragma unroll
    for (int j = 0; j < 6; ++j) {
      const unsigned n = 64 * j + lane, row = n / 24, c = n % 24;
      s.hoff[j] = (16 * w + row) * 768 + (((c & 24) | ((c & 7) ^ fsw(row))) << 4);
    }
#pragma unroll
    for (int j = 0; j < 3; ++j) {
      const unsigned n = 64 * j + lane, rho = 8 * w + n / 24, c = n % 24, ii = rho & 15;
      const unsigned px = 32 * (ii >> 3) + 8 * ((ii >> 2) & 1) + 4 * (rho >> 4) + (ii & 3);
      s.loff[j] = px * 384 + (((c & 24) | ((c & 7) ^ fsw(ii))) << 4);
    }
    issue_lo(s, lb, 0, 0, area(0), w);
    issue_lo(s, lb, 0, 1, area(1), w);
    const unsigned wrow = 16 * (i >> 2) + (i & 3);
    {
      const char* b0 = wlat + w * 96 + wrow * 384 + G * 16;
      const char* b2 = wlat + w * 96 + (wrow + 4 * (G >> 1)) * 384 + 64 + (G & 1) * 16;
#pragma unroll
      for (int ks = 0; ks < 6; ++ks) {
        const int ro = (64 * (ks >> 1) + 8 * (ks & 1)) * 384;
        s.W[0][ks] = *(const GAS v4u*)(b0 + ro);
        s.W[1][ks] = *(const GAS v4u*)(b0 + ro + 4 * 384);
        s.W[2][ks] = *(const GAS v4u*)(b2 + ro);
      }
    }
    const int r = i >> 2, p = i & 3;
#pragma unroll
    for (int ct = 0; ct < 3; ++ct) {
      const unsigned c = 6 * w + 2 * ct + (p >> 1), f = 4 * (G & 1) + 2 * (r >> 1);
      s.aF[ct][0] = L_F + (G >> 1) * 49152 + (8 * (G & 1) + r) * 384 + (((c & 24) | ((c & 7) ^ f)) << 4) + (p & 1) * 8;
      s.aF[ct][1] = (s.aF[ct][0] ^ 16) + 1536;
    }
#pragma unroll
    for (int t = 0; t < 4; ++t) s.aP[t] = L_P + (32 * (G >> 1) + 8 * (G & 1) + r) * 128 + (((2 * (t ^ w) + (p >> 1)) ^ r ^ (4 * (G & 1))) << 4) + (p & 1) * 8;
    s.aSig = L_SIG + (i & 1) * 128 + (G >> 1) * 64 + (G & 1) * 16;
    s.aLo = i * 384 + (((2 * G) ^ fsw(i)) << 4);
    s.aT = (G >> 1) * 6144 + (48 * w + (i ^ (4 * (w & 1)))) * 32 + (((G & 1) ^ ((i >> 2) & 1)) << 4);
#pragma unroll
    for (int t = 0; t < 4; ++t)
#pragma unroll
      for (int ct = 0; ct < 3; ++ct) s.U[t][ct] = z;
    s.l = z; s.M = 0.f;
    {
      const Sel sel = make_sel(G, i);
      transpose_w(s.W, sel);
    }
    bar_vm<0>();
    issue_hi(s, hb, 2, w);
    lat_pair(s, s.latB[0], area(0), area(2));
    bar();
    issue_hi(s, hb, 3, w);
    lat_pair(s, s.latB[1], area(1), area(0));
    bar();
#pragma unroll 1
    for (int g = 0; g < 3; ++g) {
      const int k = 4 * g;
      const unsigned r0 = area(g), r1 = area(g == 2 ? 0 : g + 1), r2 = area(g == 0 ? 2 : g - 1);
      issue_lo(s, lb, g + 1, 0, r1, w);
      if (g > 0) { issue_hi(s, hb, k + 2, w); pv_check(s, k - 1, w); pv_tile(s, 3); }
      bar_vm<9>();
      issue_lo(s, lb, g + 1, 1, r2, w);
      if (g > 0) { issue_hi(s, hb, k + 3, w); pv_lo(s, s.latA); }
#pragma unroll
      for (int a = 0; a < 2; ++a)
#pragma unroll
        for (int ct = 0; ct < 3; ++ct) s.latA[a][ct] = s.latB[a][ct];
      pv_check(s, k, w); pv_tile(s, 0);
      if (g == 0) asm volatile("s_waitcnt vmcnt(3)" ::: "memory");
      bar_vm<9>();
      issue_hi(s, hb, k + 4, w);
      pv_check(s, k + 1, w); pv_tile(s, 1);
      lat_pair(s, s.latB[0], r1, r0);
      bar_vm<6>();
      issue_hi(s, hb, k + 5, w);
      pv_check(s, k + 2, w); pv_tile(s, 2);
      lat_pair(s, s.latB[1], r2, r1);
      bar_vm<6>();
    }
    {
      const int ln = lane_now(), G2 = ln >> 4, i2 = ln & 15, wr = 16 * (i2 >> 2) + (i2 & 3);
      const char* b0 = wv + w * 18432;
      const unsigned o0 = wr * 96 + G2 * 16, o2 = (wr + 4 * (G2 >> 1)) * 96 + 64 + (G2 & 1) * 16;
#pragma unroll
      for (int ks = 0; ks < 6; ++ks) {
        const char* b = b0 + (64 * (ks >> 1) + 8 * (ks & 1)) * 96;
        asm volatile("global_load_dwordx4 %0, %1, %2" : "=v"(s.W[0][ks]) : "v"(o0), "s"(b));
        asm volatile("global_load_dwordx4 %0, %1, %2 offset:384" : "=v"(s.W[1][ks]) : "v"(o0), "s"(b));
        asm volatile("global_load_dwordx4 %0, %1, %2" : "=v"(s.W[2][ks]) : "v"(o2), "s"(b));
      }
    }
    issue_hi(s, hb, 14, w);
    pv_check(s, 11, w); pv_tile(s, 3);
    bar_vm<24>();
    issue_hi(s, hb, 15, w);
    pv_lo(s, s.latA);
    pv_check(s, 12, w); pv_tile(s, 0);
    bar_vm<6>();
#pragma unroll
    for (int ks = 0; ks < 6; ++ks) asm volatile("" : "+v"(s.W[0][ks]), "+v"(s.W[1][ks]), "+v"(s.W[2][ks]));
    pv_check(s, 13, w); pv_tile(s, 1);
    v4f cs[3];
    {
      const int ln = lane_now();
      const Sel sel = make_sel(ln >> 4, ln & 15);
      transpose_w(s.W, sel);
#pragma unroll
      for (int dt = 0; dt < 3; ++dt) {
        cs[dt] = z;
#pragma unroll
        for (int ks = 0; ks < 6; ++ks) cs[dt] = mma(s.W[dt][ks], ones, cs[dt]);
      }
    }
    bar_vm<0>();
    pv_check(s, 14, w); pv_tile(s, 2);
    bar();
    pv_check(s, 15, w); pv_tile(s, 3);
    pv_lo(s, s.latB);
    const int ln = lane_now(), Ge = ln >> 4, ie = ln & 15;
#pragma unroll
    for (int t = 0; t < 4; ++t)
#pragma unroll
      for (int ct = 0; ct < 3; ++ct) {
        const unsigned c = 6 * w + 2 * ct + (Ge >> 1);
        *(LAS v2u*)lp(L_F + (t ^ w) * 24576 + ie * 384 + (((c & 24) | ((c & 7) ^ fsw(ie))) << 4) + (Ge & 1) * 8) =
            (v2u){pk2(s.U[t][ct][0], s.U[t][ct][1]), pk2(s.U[t][ct][2], s.U[t][ct][3])};
      }
    bar();
    v4f cx[3] = {z, z, z}, su = z;
#pragma unroll
    for (int ks = 0; ks < 6; ++ks) {
      const v4u ub = ld128(L_F + w * 24576 + ((ks & 1) ? (s.aLo ^ 16) : s.aLo) + 128 * (ks >> 1));
      su = mma(ones, ub, su);
#pragma unroll
      for (int dt = 0; dt < 3; ++dt) cx[dt] = mma(s.W[dt][ks], ub, cx[dt]);
    }
    const float mu = su[0] * (1.f / 192.f);
    float* o = part + ((size_t)(16 * w + ie) * 256 + blockIdx.x) * 64;
#pragma unroll
    for (int dt = 0; dt < 3; ++dt) {
      v4f v;
#pragma unroll
      for (int rr = 0; rr < 4; ++rr) v[rr] = cx[dt][rr] - mu * cs[dt][rr];
      *(v4f*)(o + 16 * dt + 4 * Ge) = v;
    }
    if (Ge == 0) { o[48] = s.l[0] + s.l[1]; o[49] = s.M; }
  } else {
    const int lane = lane_now(), G = lane >> 4, i = lane & 15;
    {
      unsigned hoff[6];
#pragma unroll
      for (int j = 0; j < 6; ++j) {
        const unsigned n = 64 * j + lane, row = n / 24, c = n % 24;
        hoff[j] = (16 * w + row) * 768 + (((c & 24) | ((c & 7) ^ fsw(row))) << 4);
      }
#pragma unroll
      for (int j = 0; j < 6; ++j) dma16(L_F + w * 24576 + j * 1024, hoff[j], hb);
#pragma unroll
      for (int j = 0; j < 6; ++j) dma16c(L_P + w * 6144 + j * 1024, lane * 16, Af + w * 6144 + j * 1024);
#pragma unroll
      for (int j = 0; j < 6; ++j) dma16(L_F + w * 24576 + 6144 + j * 1024, hoff[j], hb + 384);
      if (w == 0 && lane < 2) *(LAS unsigned*)lp(L_FLAG + lane * 4) = 0u;
    }
    bar_vm<6>();
    v4u A[4][6];
#pragma unroll
    for (int qt = 0; qt < 4; ++qt)
#pragma unroll
      for (int ks = 0; ks < 6; ++ks) A[qt][ks] = ld128(L_P + (qt * 6 + ks) * 1024 + lane * 16);
    bar();
    bar_vm<0>();
    const unsigned aF = L_F + w * 24576 + i * 384 + (((2 * G) ^ fsw(i)) << 4);
    const unsigned aPw = L_P + (16 * w + i) * 128 + ((G ^ (i & 7) ^ (((i >> 3) & 1) << 2)) << 4);
    const unsigned aSw = L_SIG + (G & 1) * 128 + (16 * w + i) * 2;
    const unsigned aTr = (w >> 1) * 6144 + 512 * G + 128 * (G & 1) + 32 * (i >> 2) + 8 * (i & 3);
    const int dsel = i - 4 * G;
    v4f M[4] = {z, z, z, z}, pool[4] = {z, z, z, z}, t[4] = {z, z, z, z}, Slo[4] = {z, z, z, z};
    v4u lat[6];
#pragma unroll
    for (int ks = 0; ks < 6; ++ks) lat[ks] = (v4u){0u, 0u, 0u, 0u};
    float c1 = 0.f, c2 = 0.f, rs = 0.f;
#pragma unroll 1
    for (int k = 0; k <= 16; ++k) {
      if (k > 0) {
        const unsigned fl = __builtin_amdgcn_readfirstlane(*(LAS unsigned*)lp(L_FLAG + ((k - 1) & 1) * 4));
        if (__builtin_expect(fl == (unsigned)k, 0)) {
          const int kp = k - 1, ln = lane_now();
          const unsigned mw = L_MW + (ln >> 4) * 32;
          const float dlo = kp > 0 ? 0.f : -1.0e6f, thr = (kp & 3) == 0 ? -__builtin_inff() : 100.f;
#pragma unroll
          for (int qt = 0; qt < 4; ++qt) {
            v4f m;
#pragma unroll
            for (int r = 0; r < 4; ++r) m[r] = rowmax16(t[qt][r]);
            if ((ln & 15) == 0) *(LAS v4f*)lp(mw + w * 256 + (32 * (qt >> 1) + 4 * (qt & 1)) * 4) = m;
          }
          bar();
          v4u pw[2], qw[2];
#pragma unroll
          for (int qt = 0; qt < 4; ++qt) {
            const unsigned o = mw + (32 * (qt >> 1) + 4 * (qt & 1)) * 4;
            const v4f m0 = ld128f(o), m1 = ld128f(o + 256), m2 = ld128f(o + 512), m3 = ld128f(o + 768);
            float pv[4];
#pragma unroll
            for (int r = 0; r < 4; ++r) {
              const float d = fminf(fmaxf(ceilf(fmaxf(fmaxf(m0[r], m1[r]), fmaxf(m2[r], m3[r]))) + HEADROOM, dlo), 1.0e6f);
              M[qt][r] += d;
              pv[r] = __builtin_amdgcn_exp2f(t[qt][r] - d) * rs;
              const float old = __builtin_amdgcn_ldexpf(pool[qt][r], -(int)d);
              pool[qt][r] = t[qt][r] >= thr ? pv[r] : old;
            }
            pw[qt >> 1][2 * (qt & 1)] = pk2(pv[0], pv[1]); pw[qt >> 1][2 * (qt & 1) + 1] = pk2(pv[2], pv[3]);
            qw[qt >> 1][2 * (qt & 1)] = pk2(pool[qt][0], pool[qt][1]); qw[qt >> 1][2 * (qt & 1) + 1] = pk2(pool[qt][2], pool[qt][3]);
          }
          st128(aPw + (kp & 1) * 8192, pw[0]); st128((aPw ^ 64) + (kp & 1) * 8192, pw[1]);
          if ((kp & 3) == 3) { st128(aPw + 16384, qw[0]); st128((aPw ^ 64) + 16384, qw[1]); }
          bar();
        }
        if (k == 16) break;
      }
      if ((k & 3) == 0) {
        const int g = k >> 2;
        const unsigned tb = ((w & 1) == 0 ? area(g == 0 ? 2 : (g == 3 ? 2 : g - 1)) : area(g == 3 ? 0 : g));
        const unsigned a0 = aTr + tb, a1 = (aTr ^ 144) + tb;
#pragma unroll
        for (int ks = 0; ks < 6; ++ks) lat[ks] = ldtr2(a0 + 2048 * (ks >> 1) + 256 * (ks & 1), a1 + 2048 * (ks >> 1) + 256 * (ks & 1));
        v4f e2 = z, e1 = z;
#pragma unroll
        for (int ks = 0; ks < 6; ++ks) { e2 = mma(lat[ks], lat[ks], e2); e1 = mma(ones, lat[ks], e1); }
#pragma unroll
        for (int qt = 0; qt < 4; ++qt) {
          Slo[qt] = z; pool[qt] = z;
#pragma unroll
          for (int ks = 0; ks < 6; ++ks) Slo[qt] = mma(A[qt][ks], lat[ks], Slo[qt]);
        }
        c2 = diag16(e2, dsel); c1 = e1[0];
      }
      v4u f[6];
      const unsigned a0 = aF + (k & 3) * 6144, a1 = (aF ^ 16) + (k & 3) * 6144;
#pragma unroll
      for (int ks = 0; ks < 6; ++ks) f[ks] = ld128(((ks & 1) ? a1 : a0) + 128 * (ks >> 1));
      v4f e2 = z, ex = z, e1 = z;
#pragma unroll
      for (int ks = 0; ks < 6; ++ks) { e2 = mma(f[ks], f[ks], e2); ex = mma(lat[ks], f[ks], ex); e1 = mma(ones, f[ks], e1); }
#pragma unroll
      for (int qt = 0; qt < 4; ++qt) {
        t[qt] = mma(A[qt][0], f[0], Slo[qt]);
#pragma unroll
        for (int ks = 1; ks < 6; ++ks) t[qt] = mma(A[qt][ks], f[ks], t[qt]);
      }
#pragma unroll
      for (int r = 0; r < 4; ++r) e2[r] = fmaf(2.f, ex[r], e2[r]);
      const float mean = (e1[0] + c1) * (1.f / 192.f);
      const float ve = fmaxf(fmaf(-mean, mean, (diag16(e2, dsel) + c2) * (1.f / 192.f)), 0.f) + 1e-5f;
      rs = __builtin_amdgcn_rsqf(ve);
      {
        const float sg = ve * rs;
        const unsigned hi = pk2(sg, 0.f);
        const unsigned lo = pk2(sg - __uint_as_float(hi << 16), 0.f);
        *(LAS u16*)lp(aSw + (k & 1) * 256) = (u16)((G & 1) ? lo : hi);
      }
      v4u pw[2];
#pragma unroll
      for (int qt = 0; qt < 4; ++qt) {
        float pv[4];
#pragma unroll
        for (int r = 0; r < 4; ++r) {
          t[qt][r] = fmaf(t[qt][r], rs, -M[qt][r]);
          pv[r] = __builtin_amdgcn_exp2f(t[qt][r]) * rs;
          pool[qt][r] += pv[r];
        }
        pw[qt >> 1][2 * (qt & 1)] = pk2(pv[0], pv[1]); pw[qt >> 1][2 * (qt & 1) + 1] = pk2(pv[2], pv[3]);
      }
      st128(aPw + (k & 1) * 8192, pw[0]); st128((aPw ^ 64) + (k & 1) * 8192, pw[1]);
      const unsigned al = (pw[0][0] | pw[0][1] | pw[0][2] | pw[0][3] | pw[1][0] | pw[1][1] | pw[1][2] | pw[1][3]) & 0x40004000u;
      if (al != 0 || k == 0) *(LAS unsigned*)lp(L_FLAG + (k & 1) * 4) = (unsigned)(k + 1);
      if ((k & 3) == 3) { st128(aPw + 16384, pack8(pool[0], pool[1])); st128((aPw ^ 64) + 16384, pack8(pool[2], pool[3])); }
      bar();
    }
    bar();
  }
}

__global__ __launch_bounds__(1024) void m2f_fin(const float* __restrict__ part, const u16* __restrict__ q, const u16* __restrict__ wo, u16* __restrict__ out) {
  __shared__ float wgt[4][256];
  __shared__ float red[16];
  __shared__ float acc[5][192];
  __shared__ float ctx[192];
  const int qi = blockIdx.x, t = threadIdx.x, h = t >> 8, b = t & 255;
  const float* p = part + ((size_t)(16 * h + qi) * 256 + b) * 64;
  const float l = p[48], M = p[49];
  float m = M;
  for (int o = 32; o >= 1; o >>= 1) m = fmaxf(m, __shfl_xor(m, o));
  if ((t & 63) == 0) red[t >> 6] = m;
  __syncthreads();
  m = fmaxf(fmaxf(red[4 * h], red[4 * h + 1]), fmaxf(red[4 * h + 2], red[4 * h + 3]));
  __syncthreads();
  const float wq = exp2f(M - m);
  float s = wq * l;
  for (int o = 32; o >= 1; o >>= 1) s += __shfl_xor(s, o);
  if ((t & 63) == 0) red[t >> 6] = s;
  __syncthreads();
  s = red[4 * h] + red[4 * h + 1] + red[4 * h + 2] + red[4 * h + 3];
  wgt[h][b] = wq / s;
  __syncthreads();
  if (t < 960) {
    const int c = t % 192, sl = t / 192, hh = c / 48, d = c % 48;
    float a = 0;
    for (int bb = sl; bb < 256; bb += 5) a += wgt[hh][bb] * part[((size_t)(16 * hh + qi) * 256 + bb) * 64 + d];
    acc[sl][c] = a;
  }
  __syncthreads();
  if (t < 192) ctx[t] = acc[0][t] + acc[1][t] + acc[2][t] + acc[3][t] + acc[4][t];
  __syncthreads();
  if (t < 960) {
    const int n = t % 192, sl = t / 192;
    float a = 0;
    for (int c = sl; c < 192; c += 5) a += ctx[c] * b2f(wo[c * 192 + n]);
    acc[sl][n] = a;
  }
  __syncthreads();
  if (t < 192) out[qi * 192 + t] = __builtin_bit_cast(u16, (__bf16)(b2f(q[qi * 192 + t]) + acc[0][t] + acc[1][t] + acc[2][t] + acc[3][t] + acc[4][t]));
}

extern "C" void kernel_launch(void* const* d_in, const int* in_sizes, int n_in, void* d_out, int out_size, void* d_ws, size_t ws_size,
                              hipStream_t stream) {
  const u16* q = (const u16*)d_in[0];
  u16* Af = (u16*)d_ws;
  float* part = (float*)((char*)d_ws + 65536);
  (void)hipFuncSetAttribute((const void*)m2f_main, hipFuncAttributeMaxDynamicSharedMemorySize, L_TOTAL);
  m2f_prep<<<64, 192, 0, stream>>>(q, (const u16*)d_in[4], (const u16*)d_in[5], (const u16*)d_in[8], Af);
  m2f_main<<<256, 512, L_TOTAL, stream>>>((const char*)d_in[1], (const char*)d_in[2], (const char*)d_in[3], (const char*)d_in[6], (const char*)Af, part);
  m2f_fin<<<16, 1024, 0, stream>>>(part, q, (const u16*)d_in[7], (u16*)d_out);
}
```

```cpp
#include <hip/hip_runtime.h>
#define WS_BASE(sz) (0)

__global__ __launch_bounds__(512, 2) void m2f_main(const void* q, const void* fhi, const void* flo, const void* wlat, const void* wq, const void* wk,
                                                   const void* wv, const void* wo, const void* wm, void* out, void* ws) {
  asm volatile(R"ASM(
.Lcode0:
  s_load_dwordx16 s[4:19], s[0:1], 0
  s_load_dwordx4 s[20:23], s[0:1], 64
  s_load_dwordx2 s[24:25], s[0:1], 80
  v_lshrrev_b32 v1, 6, v0
  v_and_b32 v204, 63, v0
  v_readfirstlane_b32 s26, v1
  v_lshrrev_b32 v205, 4, v204
  v_and_b32 v206, 15, v204
  s_and_b32 s27, s26, 3
  s_mul_i32 s42, s27, 24576
  s_mul_i32 s43, s27, 1536
  s_and_b32 s28, s2, 7
  s_lshl_b32 s28, s28, 5
  s_lshr_b32 s30, s2, 3
  s_add_u32 s30, s30, s28
  s_mul_i32 s28, s30, 393216
  s_mul_i32 s30, s30, 98304
  s_mov_b32 s32, 0
  s_cmp_eq_u32 s26, 1
  s_cbranch_scc0 .Lnocpf
  s_getpc_b64 s[40:41]
.Lpc0:
  s_sub_u32 s40, s40, .Lpc0-.Lcode0
  s_subb_u32 s41, s41, 0
  s_lshr_b32 s33, s2, 3
  s_lshl_b32 s33, s33, 10
  v_lshl_add_u32 v2, v204, 4, s33
  v_min_u32 v2, .Lcode1-.Lcode0-16, v2
  global_load_dwordx4 v[4:7], v2, s[40:41]
.Lnocpf:
  s_cmp_lt_u32 s2, 48
  s_cselect_b32 s33, s26, 1
  s_cmp_eq_u32 s33, 0
  s_cbranch_scc1 .Lprod
.Lnprod:
  s_cmp_ge_u32 s26, 4
  s_cbranch_scc0 .Ls
.Lpv:
  s_mov_b32 s33, 0x75643120
  s_mul_i32 s34, s27, 12288
  s_and_b32 s35, s27, 1
  s_and_b32 s36, s27, 2
  s_lshl_b32 s36, s36, 1
  s_or_b32 s35, s35, s36
  s_lshl_b32 s35, s35, 4
  s_mul_i32 s36, s27, 1536
  v_add_u32_e64 v207, 0, v204
  v_mul_u32_u24 v208, 2731, v207
  v_lshrrev_b32 v208, 16, v208
  v_mul_u32_u24 v209, 24, v208
  v_sub_u32 v209, v207, v209
  v_lshlrev_b32 v211, 4, v209
  v_xor_b32_e64 v211, s35, v211
  v_mul_u32_u24 v210, 384, v208
  v_add3_u32 v253, v210, v211, s36
  v_xor_b32 v254, 32, v253
  v_and_b32 v210, 14, v208
  v_lshlrev_b32_e64 v210, 1, v210
  v_bfe_u32 v210, s33, v210, 3
  v_xor_b32 v209, v209, v210
  v_lshlrev_b32 v211, 4, v209
  v_mul_u32_u24 v210, 768, v208
  v_add3_u32 v210, v210, v211, s34
  s_movk_i32 s37, 6144
  v_xad_u32 v250, v210, 64, s37
  v_subrev_u32 v247, 0, v210
  v_add_u32_e64 v207, 64, v204
  v_mul_u32_u24 v208, 2731, v207
  v_lshrrev_b32 v208, 16, v208
  v_mul_u32_u24 v209, 24, v208
  v_sub_u32 v209, v207, v209
  v_and_b32 v210, 14, v208
  v_lshlrev_b32_e64 v210, 1, v210
  v_bfe_u32 v210, s33, v210, 3
  v_xor_b32_e64 v209, v209, v210
  v_lshlrev_b32 v211, 4, v209
  v_mul_u32_u24 v210, 768, v208
  v_add3_u32 v210, v210, v211, s34
  s_movk_i32 s37, 5120
  v_xad_u32 v251, v210, 64, s37
  v_subrev_u32 v248, 1024, v210
  v_add_u32 v207, 128, v204
  v_mul_u32_u24 v208, 2731, v207
  v_lshrrev_b32 v208, 16, v208
  v_mul_u32_u24 v209, 24, v208
  v_sub_u32 v209, v207, v209
  v_and_b32 v210, 14, v208
  v_lshlrev_b32_e64 v210, 1, v210
  v_bfe_u32 v210, s33, v210, 3
  v_xor_b32 v209, v209, v210
  v_lshlrev_b32 v211, 4, v209
  v_mul_u32_u24 v210, 768, v208
  v_add3_u32 v210, v210, v211, s34
  s_movk_i32 s37, 4096
  v_xad_u32 v252, v210, 64, s37
  v_subrev_u32 v249, 2048, v210
  s_setprio 1
  s_and_b32 s33, s27, 1
  s_lshl_b32 s33, s33, 6
  v_add_u32_e64 v207, 0, v204
  v_mul_u32_u24 v208, 2731, v207
  v_lshrrev_b32 v208, 16, v208
  v_mul_u32_u24 v209, 24, v208
  v_sub_u32 v209, v207, v209
  v_and_b32 v210, 2, v208
  v_xor_b32 v209, v209, v210
  v_lshlrev_b32 v209, 4, v209
  v_mul_u32_u24 v210, 384, v208
  v_xad_u32 v210, v209, s33, v210
  v_subrev_u32 v224, 0, v210
  v_add_u32 v207, 64, v204
  v_mul_u32_u24 v208, 2731, v207
  v_lshrrev_b32 v208, 16, v208
  v_mul_u32_u24 v209, 24, v208
  v_sub_u32 v209, v207, v209
  v_and_b32 v210, 2, v208
  v_xor_b32 v209, v209, v210
  v_lshlrev_b32 v209, 4, v209
  v_mul_u32_u24 v210, 384, v208
  v_xad_u32 v210, v209, s33, v210
  v_subrev_u32 v225, 1024, v210
  v_add_u32 v207, 128, v204
  v_mul_u32_u24 v208, 2731, v207
  v_lshrrev_b32 v208, 16, v208
  v_mul_u32_u24 v209, 24, v208
  v_sub_u32 v209, v207, v209
  v_and_b32 v210, 2, v208
  v_xor_b32 v209, v209, v210
  v_lshlrev_b32 v209, 4, v209
  v_mul_u32_u24 v210, 384, v208
  v_xad_u32 v210, v209, s33, v210
  v_subrev_u32 v226, 2048, v210
  s_waitcnt lgkmcnt(0)
  s_add_u32 s28, s6, s28
  s_addc_u32 s29, s7, 0
  s_add_u32 s30, s8, s30
  s_addc_u32 s31, s9, 0
  s_mul_i32 s33, s27, 6144
  s_add_u32 s10, s10, s33
  s_addc_u32 s11, s11, 0
  s_cmp_lt_u32 s2, 48
  s_cbranch_scc0 .Lh0p
  s_barrier
.Lh0p:
  s_add_u32 m0, s43, 133120
  s_mov_b32 exec_hi, 0xffff
  global_load_lds_dwordx4 v253, s[30:31] offset:0 nt
  global_load_lds_dwordx4 v254, s[30:31] offset:768 nt
  s_mov_b32 exec_hi, -1
  s_add_u32 s30, s30, 0x1800
  s_addc_u32 s31, s31, 0x0
  s_add_u32 m0, s43, 139264
  s_mov_b32 exec_hi, 0xffff
  global_load_lds_dwordx4 v253, s[30:31] offset:0 nt
  global_load_lds_dwordx4 v254, s[30:31] offset:768 nt
  s_mov_b32 exec_hi, -1
  s_add_u32 s30, s30, 0x1800
  s_addc_u32 s31, s31, 0x0
  s_add_u32 m0, s43, 145408
  s_mov_b32 exec_hi, 0xffff
  global_load_lds_dwordx4 v253, s[30:31] offset:0 nt
  global_load_lds_dwordx4 v254, s[30:31] offset:768 nt
  s_mov_b32 exec_hi, -1
  s_add_u32 s30, s30, 0x1800
  s_addc_u32 s31, s31, 0x0
  s_add_u32 m0, s43, 151552
  s_mov_b32 exec_hi, 0xffff
  global_load_lds_dwordx4 v253, s[30:31] offset:0 nt
  global_load_lds_dwordx4 v254, s[30:31] offset:768 nt
  s_mov_b32 exec_hi, -1
  s_add_u32 s30, s30, 0x1800
  s_addc_u32 s31, s31, 0x0
  s_add_u32 m0, s42, 6144
  s_nop 0
  global_load_lds_dwordx4 v224, s[10:11] offset:0
  global_load_lds_dwordx4 v225, s[10:11] offset:1024
  global_load_lds_dwordx4 v226, s[10:11] offset:2048
  s_add_u32 s10, s10, 0xc00
  s_addc_u32 s11, s11, 0x0
  s_add_u32 m0, s42, 9216
  s_nop 0
  global_load_lds_dwordx4 v224, s[10:11] offset:0
  global_load_lds_dwordx4 v225, s[10:11] offset:1024
  global_load_lds_dwordx4 v226, s[10:11] offset:2048
  s_add_u32 s10, s10, 0x5400
  s_addc_u32 s11, s11, 0x0
  v_lshrrev_b32_e64 v208, 2, v206
  v_mul_u32_u24 v207, 384, v208
  v_mul_u32_u24 v210, 24576, v205
  v_add_u32 v207, v207, v210
  v_and_b32 v210, 3, v206
  v_lshl_add_u32 v207, v210, 3, v207
  v_lshrrev_b32 v208, 1, v208
  v_and_b32 v210, 1, v205
  v_lshl_add_u32 v208, v210, 1, v208
  s_mul_i32 s33, s27, 3
  s_add_u32 s34, s33, 0
  v_xor_b32_e64 v210, s34, v208
  v_lshl_add_u32 v228, v210, 5, v207
  s_add_u32 s34, s33, 1
  v_xor_b32 v210, s34, v208
  v_lshl_add_u32 v229, v210, 5, v207
  s_add_u32 s34, s33, 2
  v_xor_b32 v210, s34, v208
  v_lshl_add_u32 v230, v210, 5, v207
  v_lshrrev_b32 v211, 2, v206
  v_and_b32 v212, 3, v206
  v_and_b32_e64 v207, 1, v205
  v_lshl_add_u32 v208, v207, 3, v211
  v_mul_u32_u24 v209, 384, v208
  v_lshrrev_b32_e64 v210, 1, v205
  v_mul_u32_u24 v242, 49152, v210
  v_add_u32 v209, v209, v242
  v_and_b32 v242, 1, v212
  v_lshl_add_u32 v209, v242, 3, v209
  v_lshl_add_u32 v208, v210, 5, v208
  v_lshlrev_b32_e64 v208, 7, v208
  v_lshl_add_u32 v208, v242, 3, v208
  v_lshlrev_b32 v207, 2, v207
  v_and_b32 v210, 2, v211
  v_or_b32_e64 v210, v210, v207
  v_lshrrev_b32 v212, 1, v212
  s_mul_i32 s33, s27, 6
  v_add3_u32 v243, v212, s33, 0
  v_xor_b32_e64 v243, v243, v210
  v_lshl_add_u32 v232, v243, 4, v209
  v_xor_b32_e64 v243, 16, v232
  v_add_u32 v233, 1536, v243
  v_add3_u32 v243, v212, s33, 2
  v_xor_b32_e64 v243, v243, v210
  v_lshl_add_u32 v234, v243, 4, v209
  v_xor_b32_e64 v243, 16, v234
  v_add_u32 v235, 1536, v243
  v_add3_u32 v243, v212, s33, 4
  v_xor_b32_e64 v243, v243, v210
  v_lshl_add_u32 v236, v243, 4, v209
  v_xor_b32_e64 v243, 16, v236
  v_add_u32 v237, 1536, v243
  v_xor_b32 v207, v207, v211
  v_xor_b32 v207, v207, v212
  v_add_u32 v208, 98304, v208
  s_xor_b32 s33, s27, 0
  s_lshl_b32 s33, s33, 1
  v_xor_b32_e64 v209, s33, v207
  v_lshl_add_u32 v238, v209, 4, v208
  s_xor_b32 s33, s27, 1
  s_lshl_b32 s33, s33, 1
  v_xor_b32_e64 v209, s33, v207
  v_lshl_add_u32 v239, v209, 4, v208
  s_xor_b32 s33, s27, 2
  s_lshl_b32 s33, s33, 1
  v_xor_b32_e64 v209, s33, v207
  v_lshl_add_u32 v240, v209, 4, v208
  s_xor_b32 s33, s27, 3
  s_lshl_b32 s33, s33, 1
  v_xor_b32_e64 v209, s33, v207
  v_lshl_add_u32 v241, v209, 4, v208
  v_and_b32 v207, 1, v206
  v_lshlrev_b32 v207, 7, v207
  v_lshrrev_b32_e64 v208, 1, v205
  v_lshl_add_u32 v207, v208, 6, v207
  v_and_b32_e64 v209, 1, v205
  v_lshl_add_u32 v207, v209, 4, v207
  v_add_u32 v242, 157696, v207
  v_and_b32 v207, 1, v211
  v_lshlrev_b32 v207, 1, v207
  v_xor_b32 v207, v207, v205
  v_lshlrev_b32 v208, 3, v207
  s_and_b32 s33, s27, 1
  s_lshl_b32 s33, s33, 2
  v_xor_b32_e64 v207, s33, v206
  v_lshl_add_u32 v208, v207, 5, v208
  s_add_u32 s33, s43, 133120
  v_add_u32_e64 v245, s33, v208
  v_xor_b32 v246, 128, v245
  s_mov_b32 s33, 0x75643120
  v_and_b32 v207, 14, v206
  v_lshlrev_b32 v207, 1, v207
  v_bfe_u32 v207, s33, v207, 3
  v_lshlrev_b32 v208, 1, v205
  v_xor_b32 v207, v207, v208
  v_lshlrev_b32_e64 v207, 4, v207
  v_mul_u32_u24 v208, 384, v206
  s_mov_b32 s33, 133120
  v_add3_u32 v243, v207, v208, s33
  v_xor_b32_e64 v244, 16, v243
  s_mov_b32 s44, 0x07060302
  v_mov_b32 v120, 159744
  v_mov_b32 v220, 61440
  global_load_dword v220, v220, s[24:25] sc1
  s_add_u32 m0, s42, 12288
  s_nop 0
  global_load_lds_dwordx4 v224, s[10:11] offset:0
  global_load_lds_dwordx4 v225, s[10:11] offset:1024
  global_load_lds_dwordx4 v226, s[10:11] offset:2048
  s_add_u32 s10, s10, 0xc00
  s_addc_u32 s11, s11, 0x0
  s_add_u32 m0, s42, 15360
  s_nop 0
  global_load_lds_dwordx4 v224, s[10:11] offset:0
  global_load_lds_dwordx4 v225, s[10:11] offset:1024
  global_load_lds_dwordx4 v226, s[10:11] offset:2048
  s_add_u32 s10, s10, 0x5400
  s_addc_u32 s11, s11, 0x0
  s_add_u32 m0, s42, 18432
  s_nop 0
  global_load_lds_dwordx4 v224, s[10:11] offset:0
  global_load_lds_dwordx4 v225, s[10:11] offset:1024
  global_load_lds_dwordx4 v226, s[10:11] offset:2048
  s_add_u32 s10, s10, 0xc00
  s_addc_u32 s11, s11, 0x0
  s_add_u32 m0, s42, 21504
  s_nop 0
  global_load_lds_dwordx4 v224, s[10:11] offset:0
  global_load_lds_dwordx4 v225, s[10:11] offset:1024
  global_load_lds_dwordx4 v226, s[10:11] offset:2048
  s_waitcnt vmcnt(12)
  v_readfirstlane_b32 s54, v220
  s_add_u32 s54, s54, 1
  s_waitcnt vmcnt(12) lgkmcnt(0)
  s_barrier
  ds_read_b64_tr_b16 v[0:1], v228 offset:6144
  ds_read_b64_tr_b16 v[2:3], v228 offset:7680
  ds_read_b64_tr_b16 v[24:25], v229 offset:6144
  ds_read_b64_tr_b16 v[26:27], v229 offset:7680
  ds_read_b64_tr_b16 v[48:49], v230 offset:6144
  ds_read_b64_tr_b16 v[50:51], v230 offset:7680
  ds_read_b128 v[124:127], v243 offset:0
  ds_read_b128 v[128:131], v243 offset:6144
  ds_read_b128 v[172:175], v243 offset:12288
  ds_read_b128 v[176:179], v243 offset:18432
  ds_read_b64_tr_b16 v[4:5], v228 offset:9216
  ds_read_b64_tr_b16 v[6:7], v228 offset:10752
  ds_read_b64_tr_b16 v[28:29], v229 offset:9216
  ds_read_b64_tr_b16 v[30:31], v229 offset:10752
  ds_read_b64_tr_b16 v[52:53], v230 offset:9216
  ds_read_b64_tr_b16 v[54:55], v230 offset:10752
  ds_read_b128 v[132:135], v244 offset:0
  ds_read_b128 v[136:139], v244 offset:6144
  ds_read_b128 v[180:183], v244 offset:12288
  ds_read_b128 v[184:187], v244 offset:18432
  s_waitcnt lgkmcnt(13)
  v_mfma_f32_16x16x32_bf16 v[72:75], v[124:127], v[0:3], 0
  ds_read_b128 v[140:143], v243 offset:128
  v_mfma_f32_16x16x32_bf16 v[76:79], v[124:127], v[24:27], 0
  ds_read_b128 v[144:147], v243 offset:6272
  v_mfma_f32_16x16x32_bf16 v[80:83], v[124:127], v[48:51], 0
  ds_read_b128 v[188:191], v243 offset:12416
  s_waitcnt lgkmcnt(15)
  v_mfma_f32_16x16x32_bf16 v[84:87], v[128:131], v[0:3], 0
  ds_read_b128 v[192:195], v243 offset:18560
  v_mfma_f32_16x16x32_bf16 v[88:91], v[128:131], v[24:27], 0
  ds_read_b128 v[148:151], v244 offset:128
  v_mfma_f32_16x16x32_bf16 v[92:95], v[128:131], v[48:51], 0
  ds_read_b128 v[152:155], v244 offset:6272
  s_waitcnt lgkmcnt(15)
  v_mfma_f32_16x16x32_bf16 v[96:99], v[172:175], v[0:3], 0
  ds_read_b128 v[196:199], v244 offset:12416
  v_mfma_f32_16x16x32_bf16 v[100:103], v[172:175], v[24:27], 0
  ds_read_b128 v[200:203], v244 offset:18560
  v_mfma_f32_16x16x32_bf16 v[104:107], v[172:175], v[48:51], 0
  ds_read_b128 v[156:159], v243 offset:256
  v_mfma_f32_16x16x32_bf16 v[108:111], v[176:179], v[0:3], 0
  ds_read_b128 v[160:163], v243 offset:6400
  v_mfma_f32_16x16x32_bf16 v[112:115], v[176:179], v[24:27], 0
  ds_read_b128 v[204:207], v243 offset:12544
  v_mfma_f32_16x16x32_bf16 v[116:119], v[176:179], v[48:51], 0
  ds_read_b128 v[208:211], v243 offset:18688
  s_waitcnt lgkmcnt(15)
  v_mfma_f32_16x16x32_bf16 v[72:75], v[132:135], v[4:7], v[72:75]
  ds_read_b128 v[164:167], v244 offset:256
  v_mfma_f32_16x16x32_bf16 v[76:79], v[132:135], v[28:31], v[76:79]
  ds_read_b128 v[168:171], v244 offset:6400
  v_mfma_f32_16x16x32_bf16 v[80:83], v[132:135], v[52:55], v[80:83]
  ds_read_b128 v[212:215], v244 offset:12544
  s_waitcnt lgkmcnt(15)
  v_mfma_f32_16x16x32_bf16 v[84:87], v[136:139], v[4:7], v[84:87]
  ds_read_b128 v[216:219], v244 offset:18688
  v_mfma_f32_16x16x32_bf16 v[88:91], v[136:139], v[28:31], v[88:91]
  v_mfma_f32_16x16x32_bf16 v[92:95], v[136:139], v[52:55], v[92:95]
  v_mfma_f32_16x16x32_bf16 v[96:99], v[180:183], v[4:7], v[96:99]
  v_mfma_f32_16x16x32_bf16 v[100:103], v[180:183], v[28:31], v[100:103]
  v_mfma_f32_16x16x32_bf16 v[104:107], v[180:183], v[52:55], v[104:107]
  v_mfma_f32_16x16x32_bf16 v[108:111], v[184:187], v[4:7], v[108:111]
  v_mfma_f32_16x16x32_bf16 v[112:115], v[184:187], v[28:31], v[112:115]
  v_mfma_f32_16x16x32_bf16 v[116:119], v[184:187], v[52:55], v[116:119]
  s_add_u32 m0, s42, 0
  s_nop 0
  global_load_lds_dwordx4 v247, s[28:29] offset:0 nt
  global_load_lds_dwordx4 v248, s[28:29] offset:1024 nt
  global_load_lds_dwordx4 v249, s[28:29] offset:2048 nt
  s_add_u32 m0, s42, 3072
  s_nop 0
  global_load_lds_dwordx4 v250, s[28:29] offset:0 nt
  global_load_lds_dwordx4 v251, s[28:29] offset:1024 nt
  global_load_lds_dwordx4 v252, s[28:29] offset:2048 nt
  s_add_u32 s28, s28, 0x180
  s_addc_u32 s29, s29, 0x0
  s_waitcnt vmcnt(12) lgkmcnt(0)
  s_barrier
  ds_read_b64_tr_b16 v[8:9], v228 offset:12288
  ds_read_b64_tr_b16 v[10:11], v228 offset:13824
  ds_read_b64_tr_b16 v[32:33], v229 offset:12288
  ds_read_b64_tr_b16 v[34:35], v229 offset:13824
  ds_read_b64_tr_b16 v[56:57], v230 offset:12288
  ds_read_b64_tr_b16 v[58:59], v230 offset:13824
  ds_read_b64_tr_b16 v[12:13], v228 offset:15360
  ds_read_b64_tr_b16 v[14:15], v228 offset:16896
  ds_read_b64_tr_b16 v[36:37], v229 offset:15360
  ds_read_b64_tr_b16 v[38:39], v229 offset:16896
  ds_read_b64_tr_b16 v[60:61], v230 offset:15360
  ds_read_b64_tr_b16 v[62:63], v230 offset:16896
  s_waitcnt lgkmcnt(10)
  v_mfma_f32_16x16x32_bf16 v[72:75], v[140:143], v[8:11], v[72:75]
  s_waitcnt lgkmcnt(8)
  v_mfma_f32_16x16x32_bf16 v[76:79], v[140:143], v[32:35], v[76:79]
  s_waitcnt lgkmcnt(6)
  v_mfma_f32_16x16x32_bf16 v[80:83], v[140:143], v[56:59], v[80:83]
  v_mfma_f32_16x16x32_bf16 v[84:87], v[144:147], v[8:11], v[84:87]
  v_mfma_f32_16x16x32_bf16 v[88:91], v[144:147], v[32:35], v[88:91]
  v_mfma_f32_16x16x32_bf16 v[92:95], v[144:147], v[56:59], v[92:95]
  v_mfma_f32_16x16x32_bf16 v[96:99], v[188:191], v[8:11], v[96:99]
  v_mfma_f32_16x16x32_bf16 v[100:103], v[188:191], v[32:35], v[100:103]
  v_mfma_f32_16x16x32_bf16 v[104:107], v[188:191], v[56:59], v[104:107]
  v_mfma_f32_16x16x32_bf16 v[108:111], v[192:195], v[8:11], v[108:111]
  v_mfma_f32_16x16x32_bf16 v[112:115], v[192:195], v[32:35], v[112:115]
  v_mfma_f32_16x16x32_bf16 v[116:119], v[192:195], v[56:59], v[116:119]
  s_waitcnt lgkmcnt(4)
  v_mfma_f32_16x16x32_bf16 v[72:75], v[148:151], v[12:15], v[72:75]
  s_waitcnt lgkmcnt(2)
  v_mfma_f32_16x16x32_bf16 v[76:79], v[148:151], v[36:39], v[76:79]
  s_waitcnt lgkmcnt(0)
  v_mfma_f32_16x16x32_bf16 v[80:83], v[148:151], v[60:63], v[80:83]
  v_mfma_f32_16x16x32_bf16 v[84:87], v[152:155], v[12:15], v[84:87]
  v_mfma_f32_16x16x32_bf16 v[88:91], v[152:155], v[36:39], v[88:91]
  v_mfma_f32_16x16x32_bf16 v[92:95], v[152:155], v[60:63], v[92:95]
  v_mfma_f32_16x16x32_bf16 v[96:99], v[196:199], v[12:15], v[96:99]
  v_mfma_f32_16x16x32_bf16 v[100:103], v[196:199], v[36:39], v[100:103]
  v_mfma_f32_16x16x32_bf16 v[104:107], v[196:199], v[60:63], v[104:107]
  v_mfma_f32_16x16x32_bf16 v[108:111], v[200:203], v[12:15], v[108:111]
  v_mfma_f32_16x16x32_bf16 v[112:115], v[200:203], v[36:39], v[112:115]
  v_mfma_f32_16x16x32_bf16 v[116:119], v[200:203], v[60:63], v[116:119]
  s_waitcnt vmcnt(6) lgkmcnt(0)
  s_barrier
  ds_read_b64_tr_b16 v[16:17], v228 offset:18432
  ds_read_b64_tr_b16 v[18:19], v228 offset:19968
  ds_read_b64_tr_b16 v[40:41], v229 offset:18432
  ds_read_b64_tr_b16 v[42:43], v229 offset:19968
  ds_read_b64_tr_b16 v[64:65], v230 offset:18432
  ds_read_b64_tr_b16 v[66:67], v230 offset:19968
  ds_read_b64_tr_b16 v[20:21], v228 offset:21504
  ds_read_b64_tr_b16 v[22:23], v228 offset:23040
  ds_read_b64_tr_b16 v[44:45], v229 offset:21504
  ds_read_b64_tr_b16 v[46:47], v229 offset:23040
  ds_read_b64_tr_b16 v[68:69], v230 offset:21504
  ds_read_b64_tr_b16 v[70:71], v230 offset:23040
  s_waitcnt lgkmcnt(10)
  v_mfma_f32_16x16x32_bf16 v[72:75], v[156:159], v[16:19], v[72:75]
  s_waitcnt lgkmcnt(8)
  v_mfma_f32_16x16x32_bf16 v[76:79], v[156:159], v[40:43], v[76:79]
  s_waitcnt lgkmcnt(6)
  v_mfma_f32_16x16x32_bf16 v[80:83], v[156:159], v[64:67], v[80:83]
  v_mfma_f32_16x16x32_bf16 v[84:87], v[160:163], v[16:19], v[84:87]
  v_mfma_f32_16x16x32_bf16 v[88:91], v[160:163], v[40:43], v[88:91]
  v_mfma_f32_16x16x32_bf16 v[92:95], v[160:163], v[64:67], v[92:95]
  v_mfma_f32_16x16x32_bf16 v[96:99], v[204:207], v[16:19], v[96:99]
  v_mfma_f32_16x16x32_bf16 v[100:103], v[204:207], v[40:43], v[100:103]
  v_mfma_f32_16x16x32_bf16 v[104:107], v[204:207], v[64:67], v[104:107]
  v_mfma_f32_16x16x32_bf16 v[108:111], v[208:211], v[16:19], v[108:111]
  v_mfma_f32_16x16x32_bf16 v[112:115], v[208:211], v[40:43], v[112:115]
  v_mfma_f32_16x16x32_bf16 v[116:119], v[208:211], v[64:67], v[116:119]
  s_cmp_lt_u32 s2, 48
  s_cbranch_scc1 .Lpl0
  s_add_u32 m0, s42, 6144
  s_nop 0
  global_load_lds_dwordx4 v247, s[28:29] offset:0 nt
  global_load_lds_dwordx4 v248, s[28:29] offset:1024 nt
  global_load_lds_dwordx4 v249, s[28:29] offset:2048 nt
  s_add_u32 m0, s42, 9216
  s_nop 0
  global_load_lds_dwordx4 v250, s[28:29] offset:0 nt
  global_load_lds_dwordx4 v251, s[28:29] offset:1024 nt
  global_load_lds_dwordx4 v252, s[28:29] offset:2048 nt
  s_add_u32 s28, s28, 0x2fe80
  s_addc_u32 s29, s29, 0x0
.Lpl0:
  s_waitcnt lgkmcnt(4)
  v_mfma_f32_16x16x32_bf16 v[72:75], v[164:167], v[20:23], v[72:75]
  s_waitcnt lgkmcnt(2)
  v_mfma_f32_16x16x32_bf16 v[76:79], v[164:167], v[44:47], v[76:79]
  s_waitcnt lgkmcnt(0)
  v_mfma_f32_16x16x32_bf16 v[80:83], v[164:167], v[68:71], v[80:83]
  v_subrev_u32 v243, 18432, v243
  v_subrev_u32 v244, 18432, v244
  v_mfma_f32_16x16x32_bf16 v[84:87], v[168:171], v[20:23], v[84:87]
  v_mfma_f32_16x16x32_bf16 v[88:91], v[168:171], v[44:47], v[88:91]
  v_mfma_f32_16x16x32_bf16 v[92:95], v[168:171], v[68:71], v[92:95]
  v_mfma_f32_16x16x32_bf16 v[96:99], v[212:215], v[20:23], v[96:99]
  v_mfma_f32_16x16x32_bf16 v[100:103], v[212:215], v[44:47], v[100:103]
  v_mfma_f32_16x16x32_bf16 v[104:107], v[212:215], v[68:71], v[104:107]
  v_mfma_f32_16x16x32_bf16 v[108:111], v[216:219], v[20:23], v[108:111]
  v_mfma_f32_16x16x32_bf16 v[112:115], v[216:219], v[44:47], v[112:115]
  v_mfma_f32_16x16x32_bf16 v[116:119], v[216:219], v[68:71], v[116:119]
  v_cvt_pk_bf16_f32 v124, v72, v73
  v_cvt_pk_bf16_f32 v125, v74, v75
  v_cvt_pk_bf16_f32 v126, v76, v77
  v_cvt_pk_bf16_f32 v127, v78, v79
  v_cvt_pk_bf16_f32 v128, v80, v81
  v_cvt_pk_bf16_f32 v129, v82, v83
  ds_write_b64 v245, v[124:125] offset:0
  ds_write_b64 v246, v[126:127] offset:512
  ds_write_b64 v245, v[128:129] offset:1024
  v_cvt_pk_bf16_f32 v130, v84, v85
  v_cvt_pk_bf16_f32 v131, v86, v87
  v_cvt_pk_bf16_f32 v132, v88, v89
  v_cvt_pk_bf16_f32 v133, v90, v91
  v_cvt_pk_bf16_f32 v134, v92, v93
  v_cvt_pk_bf16_f32 v135, v94, v95
  ds_write_b64 v245, v[130:131] offset:6144
  ds_write_b64 v246, v[132:133] offset:6656
  ds_write_b64 v245, v[134:135] offset:7168
  v_cvt_pk_bf16_f32 v136, v96, v97
  v_cvt_pk_bf16_f32 v137, v98, v99
  v_cvt_pk_bf16_f32 v138, v100, v101
  v_cvt_pk_bf16_f32 v139, v102, v103
  v_cvt_pk_bf16_f32 v140, v104, v105
  v_cvt_pk_bf16_f32 v141, v106, v107
  ds_write_b64 v245, v[136:137] offset:12288
  ds_write_b64 v246, v[138:139] offset:12800
  ds_write_b64 v245, v[140:141] offset:13312
  v_cvt_pk_bf16_f32 v142, v108, v109
  v_cvt_pk_bf16_f32 v143, v110, v111
  v_cvt_pk_bf16_f32 v144, v112, v113
  v_cvt_pk_bf16_f32 v145, v114, v115
  v_cvt_pk_bf16_f32 v146, v116, v117
  v_cvt_pk_bf16_f32 v147, v118, v119
  ds_write_b64 v245, v[142:143] offset:18432
  ds_write_b64 v246, v[144:145] offset:18944
  ds_write_b64 v245, v[146:147] offset:19456
  s_cmp_lt_u32 s2, 48
  s_cbranch_scc0 .Lpl1
  s_waitcnt vmcnt(0)
.Lpl1:
  s_waitcnt vmcnt(6) lgkmcnt(0)
  s_barrier
  s_mov_b32 s33, 0
.Lpw:
  s_sleep 1
  ds_read_b32 v122, v120 offset:16
  s_add_u32 s33, s33, 1
  s_waitcnt lgkmcnt(0)
  v_readfirstlane_b32 s34, v122
  s_cmp_ge_u32 s33, 0x100000
  s_cbranch_scc1 .Lpwd
  s_cmp_lt_u32 s34, 4
  s_cbranch_scc1 .Lpw
.Lpwd:
  s_add_u32 m0, s43, 114688
  s_mov_b32 exec_hi, 0xffff
  global_load_lds_dwordx4 v253, s[30:31] offset:0 nt
  global_load_lds_dwordx4 v254, s[30:31] offset:768 nt
  s_mov_b32 exec_hi, -1
  s_add_u32 s30, s30, 0x1800
  s_addc_u32 s31, s31, 0x0
  s_cmp_lt_u32 s2, 48
  s_cbranch_scc0 .Lpl2
  s_add_u32 m0, s42, 6144
  s_nop 0
  global_load_lds_dwordx4 v247, s[28:29] offset:0 nt
  global_load_lds_dwordx4 v248, s[28:29] offset:1024 nt
  global_load_lds_dwordx4 v249, s[28:29] offset:2048 nt
  s_add_u32 m0, s42, 9216
  s_nop 0
  global_load_lds_dwordx4 v250, s[28:29] offset:0 nt
  global_load_lds_dwordx4 v251, s[28:29] offset:1024 nt
  global_load_lds_dwordx4 v252, s[28:29] offset:2048 nt
  s_add_u32 s28, s28, 0x2fe80
  s_addc_u32 s29, s29, 0x0
.Lpl2:
  s_add_u32 m0, s43, 120832
  s_mov_b32 exec_hi, 0xffff
  global_load_lds_dwordx4 v253, s[30:31] offset:0 nt
  global_load_lds_dwordx4 v254, s[30:31] offset:768 nt
  s_mov_b32 exec_hi, -1
  s_add_u32 s30, s30, 0x1800
  s_addc_u32 s31, s31, 0x0
  s_add_u32 m0, s43, 126976
  s_mov_b32 exec_hi, 0xffff
  global_load_lds_dwordx4 v253, s[30:31] offset:0 nt
  global_load_lds_dwordx4 v254, s[30:31] offset:768 nt
  s_mov_b32 exec_hi, -1
  s_add_u32 s30, s30, 0x1800
  s_addc_u32 s31, s31, 0x0
  s_add_u32 m0, s42, 12288
  s_nop 0
  global_load_lds_dwordx4 v247, s[28:29] offset:0 nt
  global_load_lds_dwordx4 v248, s[28:29] offset:1024 nt
  global_load_lds_dwordx4 v249, s[28:29] offset:2048 nt
  s_add_u32 s43, s43, 114688
  s_add_u32 s46, s43, 18432
  s_add_u32 s55, s43, 12288
  s_mov_b32 s92, s55
  s_cmp_lt_u32 s2, 48
  s_cbranch_scc1 .Lpl3
  s_waitcnt vmcnt(7)
.Lpl3:
  s_waitcnt vmcnt(13) lgkmcnt(0)
  s_barrier
  ds_read_b128 v[124:127], v243 offset:0
  ds_read_b128 v[128:131], v244 offset:0
  ds_read_b128 v[132:135], v243 offset:128
  ds_read_b128 v[136:139], v244 offset:128
  ds_read_b128 v[140:143], v243 offset:256
  ds_read_b128 v[144:147], v244 offset:256
  s_waitcnt lgkmcnt(5)
  v_mfma_f32_16x16x32_bf16 v[148:151], v[124:127], v[0:3], 0
  s_add_u32 m0, s42, 15360
  s_nop 0
  global_load_lds_dwordx4 v250, s[28:29] offset:0 nt
  v_mfma_f32_16x16x32_bf16 v[152:155], v[124:127], v[24:27], 0
  v_mfma_f32_16x16x32_bf16 v[156:159], v[124:127], v[48:51], 0
  global_load_lds_dwordx4 v251, s[28:29] offset:1024 nt
  s_waitcnt lgkmcnt(4)
  v_mfma_f32_16x16x32_bf16 v[148:151], v[128:131], v[4:7], v[148:151]
  s_add_u32 s92, s92, 6144
  v_mfma_f32_16x16x32_bf16 v[152:155], v[128:131], v[28:31], v[152:155]
  global_load_lds_dwordx4 v252, s[28:29] offset:2048 nt
  s_add_u32 s28, s28, 0x180
  s_addc_u32 s29, s29, 0x0
  s_cmp_eq_u32 s92, s46
  s_cselect_b32 s92, s43, s92
  v_mfma_f32_16x16x32_bf16 v[156:159], v[128:131], v[52:55], v[156:159]
  s_movk_i32 s33, 6144
  s_waitcnt lgkmcnt(3)
  v_mfma_f32_16x16x32_bf16 v[148:151], v[132:135], v[8:11], v[148:151]
  s_cmp_eq_u32 s92, s55
  s_cmovk_i32 s33, -12288
  v_mfma_f32_16x16x32_bf16 v[152:155], v[132:135], v[32:35], v[152:155]
  v_add_u32 v243, s33, v243
  v_mfma_f32_16x16x32_bf16 v[156:159], v[132:135], v[56:59], v[156:159]
  v_add_u32 v244, s33, v244
  s_waitcnt lgkmcnt(2)
  v_mfma_f32_16x16x32_bf16 v[148:151], v[136:139], v[12:15], v[148:151]
  v_mfma_f32_16x16x32_bf16 v[152:155], v[136:139], v[36:39], v[152:155]
  v_mfma_f32_16x16x32_bf16 v[156:159], v[136:139], v[60:63], v[156:159]
  s_waitcnt lgkmcnt(1)
  v_mfma_f32_16x16x32_bf16 v[148:151], v[140:143], v[16:19], v[148:151]
  v_mfma_f32_16x16x32_bf16 v[152:155], v[140:143], v[40:43], v[152:155]
  v_mfma_f32_16x16x32_bf16 v[156:159], v[140:143], v[64:67], v[156:159]
  s_waitcnt lgkmcnt(0)
  v_mfma_f32_16x16x32_bf16 v[148:151], v[144:147], v[20:23], v[148:151]
  v_mfma_f32_16x16x32_bf16 v[152:155], v[144:147], v[44:47], v[152:155]
  v_mfma_f32_16x16x32_bf16 v[156:159], v[144:147], v[68:71], v[156:159]
  s_nop 0
  s_nop 4
  v_cvt_pk_bf16_f32 v160, v148, v149
  v_cvt_pk_bf16_f32 v161, v150, v151
  v_cvt_pk_bf16_f32 v162, v152, v153
  v_cvt_pk_bf16_f32 v163, v154, v155
  v_cvt_pk_bf16_f32 v164, v156, v157
  v_cvt_pk_bf16_f32 v165, v158, v159
  ds_write_b64 v245, v[160:161] offset:0
  ds_write_b64 v246, v[162:163] offset:512
  ds_write_b64 v245, v[164:165] offset:1024
  s_waitcnt vmcnt(8) lgkmcnt(0)
  s_barrier
  s_add_u32 s32, s32, 1
  ds_read_b128 v[124:127], v243 offset:0
  ds_read_b128 v[128:131], v244 offset:0
  ds_read_b128 v[132:135], v243 offset:128
  ds_read_b128 v[136:139], v244 offset:128
  ds_read_b128 v[140:143], v243 offset:256
  ds_read_b128 v[144:147], v244 offset:256
  s_mov_b32 m0, s92
  s_mov_b32 exec_hi, 0xffff
  global_load_lds_dwordx4 v253, s[30:31] offset:0 nt
  global_load_lds_dwordx4 v254, s[30:31] offset:768 nt
  s_mov_b32 exec_hi, -1
  s_add_u32 s30, s30, 0x1800
  s_addc_u32 s31, s31, 0x0
  s_waitcnt lgkmcnt(5)
  v_mfma_f32_16x16x32_bf16 v[148:151], v[124:127], v[0:3], 0
  ds_read_b128 v[200:203], v242 offset:0
  ds_read_b64_tr_b16 v[172:173], v238 offset:0
  v_mfma_f32_16x16x32_bf16 v[152:155], v[124:127], v[24:27], 0
  ds_read_b64_tr_b16 v[174:175], v240 offset:512
  ds_read_b64_tr_b16 v[188:189], v232 offset:0
  v_mfma_f32_16x16x32_bf16 v[156:159], v[124:127], v[48:51], 0
  ds_read_b64_tr_b16 v[190:191], v233 offset:0
  ds_read_b64_tr_b16 v[192:193], v234 offset:0
  s_waitcnt lgkmcnt(10)
  v_mfma_f32_16x16x32_bf16 v[148:151], v[128:131], v[4:7], v[148:151]
  ds_read_b64_tr_b16 v[194:195], v235 offset:0
  ds_read_b64_tr_b16 v[196:197], v236 offset:0
  v_mfma_f32_16x16x32_bf16 v[152:155], v[128:131], v[28:31], v[152:155]
  s_add_u32 m0, s42, 18432
  s_nop 0
  global_load_lds_dwordx4 v247, s[28:29] offset:0 nt
  ds_read_b64_tr_b16 v[198:199], v237 offset:0
  ds_read_b64_tr_b16 v[176:177], v239 offset:0
  v_mfma_f32_16x16x32_bf16 v[156:159], v[128:131], v[52:55], v[156:159]
  ds_read_b64_tr_b16 v[178:179], v241 offset:512
  ds_read_b64_tr_b16 v[180:181], v240 offset:0
  s_waitcnt lgkmcnt(15)
  v_mfma_f32_16x16x32_bf16 v[148:151], v[132:135], v[8:11], v[148:151]
  ds_read_b64_tr_b16 v[182:183], v238 offset:512
  ds_read_b64_tr_b16 v[184:185], v241 offset:0
  v_mfma_f32_16x16x32_bf16 v[152:155], v[132:135], v[32:35], v[152:155]
  ds_read_b64_tr_b16 v[186:187], v239 offset:512
  s_add_u32 s92, s92, 6144
  v_mfma_f32_16x16x32_bf16 v[156:159], v[132:135], v[56:59], v[156:159]
  global_load_lds_dwordx4 v248, s[28:29] offset:1024 nt
  s_cmp_eq_u32 s92, s46
  s_cselect_b32 s92, s43, s92
  s_movk_i32 s33, 6144
  s_waitcnt lgkmcnt(15)
  v_mfma_f32_16x16x32_bf16 v[148:151], v[136:139], v[12:15], v[148:151]
  s_cmp_eq_u32 s92, s55
  s_cmovk_i32 s33, -12288
  v_add_u32_e64 v243, s33, v243
  v_mfma_f32_16x16x32_bf16 v[152:155], v[136:139], v[36:39], v[152:155]
  v_add_u32_e64 v244, s33, v244
  v_mfma_f32_16x16x32_bf16 v[156:159], v[136:139], v[60:63], v[156:159]
  v_mfma_f32_16x16x32_bf16 v[148:151], v[140:143], v[16:19], v[148:151]
  global_load_lds_dwordx4 v249, s[28:29] offset:2048 nt
  v_mfma_f32_16x16x32_bf16 v[152:155], v[140:143], v[40:43], v[152:155]
  v_mfma_f32_16x16x32_bf16 v[156:159], v[140:143], v[64:67], v[156:159]
  v_mfma_f32_16x16x32_bf16 v[148:151], v[144:147], v[20:23], v[148:151]
  v_mfma_f32_16x16x32_bf16 v[152:155], v[144:147], v[44:47], v[152:155]
  s_add_u32 m0, s42, 21504
  s_nop 0
  global_load_lds_dwordx4 v250, s[28:29] offset:0 nt
  v_mfma_f32_16x16x32_bf16 v[156:159], v[144:147], v[68:71], v[156:159]
  s_waitcnt lgkmcnt(10)
  v_mfma_f32_16x16x32_bf16 v[72:75], v[188:191], v[172:175], 0
  ds_read_b64_tr_b16 v[204:205], v238 offset:2048
  ds_read_b64_tr_b16 v[206:207], v240 offset:2560
  s_waitcnt lgkmcnt(10)
  v_mfma_f32_16x16x32_bf16 v[76:79], v[192:195], v[172:175], 0
  ds_read_b128 v[168:171], v242 offset:32
  ds_read_b64_tr_b16 v[220:221], v232 offset:24576
  s_waitcnt lgkmcnt(10)
  v_mfma_f32_16x16x32_bf16 v[80:83], v[196:199], v[172:175], 0
  global_load_lds_dwordx4 v251, s[28:29] offset:1024 nt
  ds_read_b64_tr_b16 v[222:223], v233 offset:24576
  v_cvt_pk_bf16_f32 v160, v148, v149
  v_mfma_f32_16x16x32_bf16 v[120:123], v[172:175], v[200:203], 0
  v_cvt_pk_bf16_f32 v161, v150, v151
  v_cvt_pk_bf16_f32 v162, v152, v153
  s_waitcnt lgkmcnt(9)
  v_mfma_f32_16x16x32_bf16 v[84:87], v[188:191], v[176:179], 0
  v_cvt_pk_bf16_f32 v163, v154, v155
  v_cvt_pk_bf16_f32 v164, v156, v157
  v_mfma_f32_16x16x32_bf16 v[88:91], v[192:195], v[176:179], 0
  v_cvt_pk_bf16_f32 v165, v158, v159
  ds_write_b64 v245, v[160:161] offset:6144
  v_mfma_f32_16x16x32_bf16 v[92:95], v[196:199], v[176:179], 0
  global_load_lds_dwordx4 v252, s[28:29] offset:2048 nt
  s_add_u32 s28, s28, 0xfffdbe80
  s_addc_u32 s29, s29, -1
  ds_write_b64 v246, v[162:163] offset:6656
  ds_write_b64 v245, v[164:165] offset:7168
  s_waitcnt lgkmcnt(10)
  v_mfma_f32_16x16x32_bf16 v[96:99], v[188:191], v[180:183], 0
  ds_read_b64_tr_b16 v[224:225], v234 offset:24576
  ds_read_b64_tr_b16 v[226:227], v235 offset:24576
  v_mfma_f32_16x16x32_bf16 v[100:103], v[192:195], v[180:183], 0
  ds_read_b64_tr_b16 v[228:229], v236 offset:24576
  ds_read_b64_tr_b16 v[230:231], v237 offset:24576
  v_mfma_f32_16x16x32_bf16 v[104:107], v[196:199], v[180:183], 0
  ds_read_b64_tr_b16 v[208:209], v239 offset:2048
  ds_read_b64_tr_b16 v[210:211], v241 offset:2560
  s_waitcnt lgkmcnt(14)
  v_mfma_f32_16x16x32_bf16 v[108:111], v[188:191], v[184:187], 0
  ds_read_b64_tr_b16 v[212:213], v240 offset:2048
  ds_read_b64_tr_b16 v[214:215], v238 offset:2560
  v_mfma_f32_16x16x32_bf16 v[112:115], v[192:195], v[184:187], 0
  ds_read_b64_tr_b16 v[216:217], v241 offset:2048
  ds_read_b64_tr_b16 v[218:219], v239 offset:2560
  v_mfma_f32_16x16x32_bf16 v[116:119], v[196:199], v[184:187], 0
  s_waitcnt vmcnt(8) lgkmcnt(0)
  s_barrier
  s_add_u32 s32, s32, 1
.Lploop:
  v_mfma_f32_16x16x32_bf16 v[120:123], v[204:207], v[168:171], v[120:123]
  v_mfma_f32_16x16x32_bf16 v[72:75], v[220:223], v[204:207], v[72:75]
  v_mfma_f32_16x16x32_bf16 v[76:79], v[224:227], v[204:207], v[76:79]
  s_cmp_lt_u32 s32, 10
  s_cbranch_scc0 .Lnolo0
  s_mov_b32 m0, s92
  s_mov_b32 exec_hi, 0xffff
  global_load_lds_dwordx4 v253, s[30:31] offset:0 nt
  global_load_lds_dwordx4 v254, s[30:31] offset:768 nt
  s_mov_b32 exec_hi, -1
  s_add_u32 s30, s30, 0x1800
  s_addc_u32 s31, s31, 0x0
.Lnolo0:
  v_mfma_f32_16x16x32_bf16 v[80:83], v[228:231], v[204:207], v[80:83]
  v_mfma_f32_16x16x32_bf16 v[84:87], v[220:223], v[208:211], v[84:87]
  v_mfma_f32_16x16x32_bf16 v[88:91], v[224:227], v[208:211], v[88:91]
  v_mfma_f32_16x16x32_bf16 v[92:95], v[228:231], v[208:211], v[92:95]
  s_add_u32 m0, s42, 0
  s_nop 0
  global_load_lds_dwordx4 v247, s[28:29] offset:0 nt
  v_mfma_f32_16x16x32_bf16 v[96:99], v[220:223], v[212:215], v[96:99]
  ds_read_b32 v199, v242 offset:2052
  ds_read_b128 v[124:127], v243 offset:0
  ds_read_b128 v[128:131], v244 offset:0
  v_mfma_f32_16x16x32_bf16 v[100:103], v[224:227], v[212:215], v[100:103]
  ds_read_b128 v[132:135], v243 offset:128
  ds_read_b128 v[136:139], v244 offset:128
  ds_read_b128 v[140:143], v243 offset:256
  v_mfma_f32_16x16x32_bf16 v[104:107], v[228:231], v[212:215], v[104:107]
  ds_read_b128 v[144:147], v244 offset:256
  v_mfma_f32_16x16x32_bf16 v[108:111], v[220:223], v[216:219], v[108:111]
  global_load_lds_dwordx4 v248, s[28:29] offset:1024 nt
  v_mfma_f32_16x16x32_bf16 v[112:115], v[224:227], v[216:219], v[112:115]
  v_mfma_f32_16x16x32_bf16 v[116:119], v[228:231], v[216:219], v[116:119]
  s_waitcnt lgkmcnt(6)
  v_readfirstlane_b32 s45, v199
  s_cmp_eq_u32 s45, s32
  s_cbranch_scc1 .Lstub0
.Lcont0:
  s_waitcnt lgkmcnt(5)
  v_mfma_f32_16x16x32_bf16 v[148:151], v[124:127], v[0:3], 0
  ds_read_b128 v[200:203], v242 offset:256
  ds_read_b64_tr_b16 v[172:173], v238 offset:8192
  v_mfma_f32_16x16x32_bf16 v[152:155], v[124:127], v[24:27], 0
  global_load_lds_dwordx4 v249, s[28:29] offset:2048 nt
  ds_read_b64_tr_b16 v[174:175], v240 offset:8704
  ds_read_b64_tr_b16 v[188:189], v232 offset:6144
  v_mfma_f32_16x16x32_bf16 v[156:159], v[124:127], v[48:51], 0
  ds_read_b64_tr_b16 v[190:191], v233 offset:6144
  ds_read_b64_tr_b16 v[192:193], v234 offset:6144
  s_waitcnt lgkmcnt(10)
  v_mfma_f32_16x16x32_bf16 v[148:151], v[128:131], v[4:7], v[148:151]
  ds_read_b64_tr_b16 v[194:195], v235 offset:6144
  ds_read_b64_tr_b16 v[196:197], v236 offset:6144
  v_mfma_f32_16x16x32_bf16 v[152:155], v[128:131], v[28:31], v[152:155]
  ds_read_b64_tr_b16 v[198:199], v237 offset:6144
  ds_read_b64_tr_b16 v[176:177], v239 offset:8192
  v_mfma_f32_16x16x32_bf16 v[156:159], v[128:131], v[52:55], v[156:159]
  s_add_u32 m0, s42, 3072
  s_nop 0
  global_load_lds_dwordx4 v250, s[28:29] offset:0 nt
  ds_read_b64_tr_b16 v[178:179], v241 offset:8704
  ds_read_b64_tr_b16 v[180:181], v240 offset:8192
  s_waitcnt lgkmcnt(15)
  v_mfma_f32_16x16x32_bf16 v[148:151], v[132:135], v[8:11], v[148:151]
  ds_read_b64_tr_b16 v[182:183], v238 offset:8704
  ds_read_b64_tr_b16 v[184:185], v241 offset:8192
  v_mfma_f32_16x16x32_bf16 v[152:155], v[132:135], v[32:35], v[152:155]
  ds_read_b64_tr_b16 v[186:187], v239 offset:8704
  s_add_u32 s92, s92, 6144
  v_mfma_f32_16x16x32_bf16 v[156:159], v[132:135], v[56:59], v[156:159]
  s_cmp_eq_u32 s92, s46
  s_cselect_b32 s92, s43, s92
  s_movk_i32 s33, 6144
  s_waitcnt lgkmcnt(15)
  v_mfma_f32_16x16x32_bf16 v[148:151], v[136:139], v[12:15], v[148:151]
  global_load_lds_dwordx4 v251, s[28:29] offset:1024 nt
  s_cmp_eq_u32 s92, s55
  s_cmovk_i32 s33, -12288
  v_add_u32_e64 v243, s33, v243
  v_mfma_f32_16x16x32_bf16 v[152:155], v[136:139], v[36:39], v[152:155]
  v_add_u32 v244, s33, v244
  v_mfma_f32_16x16x32_bf16 v[156:159], v[136:139], v[60:63], v[156:159]
  v_mfma_f32_16x16x32_bf16 v[148:151], v[140:143], v[16:19], v[148:151]
  v_mfma_f32_16x16x32_bf16 v[152:155], v[140:143], v[40:43], v[152:155]
  global_load_lds_dwordx4 v252, s[28:29] offset:2048 nt
  s_add_u32 s28, s28, 0x180
  s_addc_u32 s29, s29, 0x0
  v_mfma_f32_16x16x32_bf16 v[156:159], v[140:143], v[64:67], v[156:159]
  v_mfma_f32_16x16x32_bf16 v[148:151], v[144:147], v[20:23], v[148:151]
  v_mfma_f32_16x16x32_bf16 v[152:155], v[144:147], v[44:47], v[152:155]
  v_mfma_f32_16x16x32_bf16 v[156:159], v[144:147], v[68:71], v[156:159]
  s_waitcnt lgkmcnt(10)
  v_mfma_f32_16x16x32_bf16 v[72:75], v[188:191], v[172:175], v[72:75]
  ds_read_b64_tr_b16 v[204:205], v238 offset:10240
  ds_read_b64_tr_b16 v[206:207], v240 offset:10752
  s_waitcnt lgkmcnt(10)
  v_mfma_f32_16x16x32_bf16 v[76:79], v[192:195], v[172:175], v[76:79]
  ds_read_b128 v[168:171], v242 offset:288
  ds_read_b64_tr_b16 v[220:221], v232 offset:30720
  s_waitcnt lgkmcnt(10)
  v_mfma_f32_16x16x32_bf16 v[80:83], v[196:199], v[172:175], v[80:83]
  ds_read_b64_tr_b16 v[222:223], v233 offset:30720
  v_cvt_pk_bf16_f32 v160, v148, v149
  v_mfma_f32_16x16x32_bf16 v[120:123], v[172:175], v[200:203], v[120:123]
  v_cvt_pk_bf16_f32 v161, v150, v151
  v_cvt_pk_bf16_f32 v162, v152, v153
  s_waitcnt lgkmcnt(9)
  v_mfma_f32_16x16x32_bf16 v[84:87], v[188:191], v[176:179], v[84:87]
  v_cvt_pk_bf16_f32 v163, v154, v155
  v_cvt_pk_bf16_f32 v164, v156, v157
  v_mfma_f32_16x16x32_bf16 v[88:91], v[192:195], v[176:179], v[88:91]
  v_cvt_pk_bf16_f32 v165, v158, v159
  ds_write_b64 v245, v[160:161] offset:12288
  v_mfma_f32_16x16x32_bf16 v[92:95], v[196:199], v[176:179], v[92:95]
  ds_write_b64 v246, v[162:163] offset:12800
  ds_write_b64 v245, v[164:165] offset:13312
  s_waitcnt lgkmcnt(10)
  v_mfma_f32_16x16x32_bf16 v[96:99], v[188:191], v[180:183], v[96:99]
  ds_read_b64_tr_b16 v[224:225], v234 offset:30720
  ds_read_b64_tr_b16 v[226:227], v235 offset:30720
  v_mfma_f32_16x16x32_bf16 v[100:103], v[192:195], v[180:183], v[100:103]
  ds_read_b64_tr_b16 v[228:229], v236 offset:30720
  ds_read_b64_tr_b16 v[230:231], v237 offset:30720
  v_mfma_f32_16x16x32_bf16 v[104:107], v[196:199], v[180:183], v[104:107]
  ds_read_b64_tr_b16 v[208:209], v239 offset:10240
  ds_read_b64_tr_b16 v[210:211], v241 offset:10752
  s_waitcnt lgkmcnt(14)
  v_mfma_f32_16x16x32_bf16 v[108:111], v[188:191], v[184:187], v[108:111]
  ds_read_b64_tr_b16 v[212:213], v240 offset:10240
  ds_read_b64_tr_b16 v[214:215], v238 offset:10752
  v_mfma_f32_16x16x32_bf16 v[112:115], v[192:195], v[184:187], v[112:115]
  ds_read_b64_tr_b16 v[216:217], v241 offset:10240
  ds_read_b64_tr_b16 v[218:219], v239 offset:10752
  v_mfma_f32_16x16x32_bf16 v[116:119], v[196:199], v[184:187], v[116:119]
  s_cmp_lt_u32 s32, 10
  s_cbranch_scc1 .Lw80
  s_waitcnt vmcnt(6)
.Lw80:
  s_waitcnt vmcnt(8) lgkmcnt(0)
  s_barrier
  s_add_u32 s32, s32, 1
  v_mfma_f32_16x16x32_bf16 v[120:123], v[204:207], v[168:171], v[120:123]
  v_mfma_f32_16x16x32_bf16 v[72:75], v[220:223], v[204:207], v[72:75]
  v_mfma_f32_16x16x32_bf16 v[76:79], v[224:227], v[204:207], v[76:79]
  s_cmp_lt_u32 s32, 10
  s_cbranch_scc0 .Lnolo1
  s_mov_b32 m0, s92
  s_mov_b32 exec_hi, 0xffff
  global_load_lds_dwordx4 v253, s[30:31] offset:0 nt
  global_load_lds_dwordx4 v254, s[30:31] offset:768 nt
  s_mov_b32 exec_hi, -1
  s_add_u32 s30, s30, 0x1800
  s_addc_u32 s31, s31, 0x0
.Lnolo1:
  v_mfma_f32_16x16x32_bf16 v[80:83], v[228:231], v[204:207], v[80:83]
  v_mfma_f32_16x16x32_bf16 v[84:87], v[220:223], v[208:211], v[84:87]
  v_mfma_f32_16x16x32_bf16 v[88:91], v[224:227], v[208:211], v[88:91]
  v_mfma_f32_16x16x32_bf16 v[92:95], v[228:231], v[208:211], v[92:95]
  s_add_u32 m0, s42, 6144
  s_nop 0
  global_load_lds_dwordx4 v247, s[28:29] offset:0 nt
  v_mfma_f32_16x16x32_bf16 v[96:99], v[220:223], v[212:215], v[96:99]
  ds_read_b32 v199, v242 offset:2048
  ds_read_b128 v[124:127], v243 offset:0
  ds_read_b128 v[128:131], v244 offset:0
  v_mfma_f32_16x16x32_bf16 v[100:103], v[224:227], v[212:215], v[100:103]
  ds_read_b128 v[132:135], v243 offset:128
  ds_read_b128 v[136:139], v244 offset:128
  ds_read_b128 v[140:143], v243 offset:256
  v_mfma_f32_16x16x32_bf16 v[104:107], v[228:231], v[212:215], v[104:107]
  ds_read_b128 v[144:147], v244 offset:256
  v_mfma_f32_16x16x32_bf16 v[108:111], v[220:223], v[216:219], v[108:111]
  global_load_lds_dwordx4 v248, s[28:29] offset:1024 nt
  v_mfma_f32_16x16x32_bf16 v[112:115], v[224:227], v[216:219], v[112:115]
  v_mfma_f32_16x16x32_bf16 v[116:119], v[228:231], v[216:219], v[116:119]
  s_waitcnt lgkmcnt(6)
  v_readfirstlane_b32 s45, v199
  s_cmp_eq_u32 s45, s32
  s_cbranch_scc1 .Lstub1
.Lcont1:
  s_waitcnt lgkmcnt(5)
  v_mfma_f32_16x16x32_bf16 v[148:151], v[124:127], v[0:3], 0
  ds_read_b128 v[200:203], v242 offset:0
  ds_read_b64_tr_b16 v[172:173], v238 offset:0
  v_mfma_f32_16x16x32_bf16 v[152:155], v[124:127], v[24:27], 0
  global_load_lds_dwordx4 v249, s[28:29] offset:2048 nt
  ds_read_b64_tr_b16 v[174:175], v240 offset:512
  ds_read_b64_tr_b16 v[188:189], v232 offset:12288
  v_mfma_f32_16x16x32_bf16 v[156:159], v[124:127], v[48:51], 0
  ds_read_b64_tr_b16 v[190:191], v233 offset:12288
  ds_read_b64_tr_b16 v[192:193], v234 offset:12288
  s_waitcnt lgkmcnt(10)
  v_mfma_f32_16x16x32_bf16 v[148:151], v[128:131], v[4:7], v[148:151]
  ds_read_b64_tr_b16 v[194:195], v235 offset:12288
  ds_read_b64_tr_b16 v[196:197], v236 offset:12288
  v_mfma_f32_16x16x32_bf16 v[152:155], v[128:131], v[28:31], v[152:155]
  ds_read_b64_tr_b16 v[198:199], v237 offset:12288
  ds_read_b64_tr_b16 v[176:177], v239 offset:0
  v_mfma_f32_16x16x32_bf16 v[156:159], v[128:131], v[52:55], v[156:159]
  s_add_u32 m0, s42, 9216
  s_nop 0
  global_load_lds_dwordx4 v250, s[28:29] offset:0 nt
  ds_read_b64_tr_b16 v[178:179], v241 offset:512
  ds_read_b64_tr_b16 v[180:181], v240 offset:0
  s_waitcnt lgkmcnt(15)
  v_mfma_f32_16x16x32_bf16 v[148:151], v[132:135], v[8:11], v[148:151]
  ds_read_b64_tr_b16 v[182:183], v238 offset:512
  ds_read_b64_tr_b16 v[184:185], v241 offset:0
  v_mfma_f32_16x16x32_bf16 v[152:155], v[132:135], v[32:35], v[152:155]
  ds_read_b64_tr_b16 v[186:187], v239 offset:512
  s_add_u32 s92, s92, 6144
  v_mfma_f32_16x16x32_bf16 v[156:159], v[132:135], v[56:59], v[156:159]
  s_cmp_eq_u32 s92, s46
  s_cselect_b32 s92, s43, s92
  s_movk_i32 s33, 6144
  s_waitcnt lgkmcnt(15)
  v_mfma_f32_16x16x32_bf16 v[148:151], v[136:139], v[12:15], v[148:151]
  global_load_lds_dwordx4 v251, s[28:29] offset:1024 nt
  s_cmp_eq_u32 s92, s55
  s_cmovk_i32 s33, -12288
  v_add_u32_e64 v243, s33, v243
  v_mfma_f32_16x16x32_bf16 v[152:155], v[136:139], v[36:39], v[152:155]
  v_add_u32 v244, s33, v244
  v_mfma_f32_16x16x32_bf16 v[156:159], v[136:139], v[60:63], v[156:159]
  v_mfma_f32_16x16x32_bf16 v[148:151], v[140:143], v[16:19], v[148:151]
  v_mfma_f32_16x16x32_bf16 v[152:155], v[140:143], v[40:43], v[152:155]
  global_load_lds_dwordx4 v252, s[28:29] offset:2048 nt
  s_add_u32 s28, s28, 0x2fe80
  s_addc_u32 s29, s29, 0x0
  v_mfma_f32_16x16x32_bf16 v[156:159], v[140:143], v[64:67], v[156:159]
  v_mfma_f32_16x16x32_bf16 v[148:151], v[144:147], v[20:23], v[148:151]
  v_mfma_f32_16x16x32_bf16 v[152:155], v[144:147], v[44:47], v[152:155]
  v_mfma_f32_16x16x32_bf16 v[156:159], v[144:147], v[68:71], v[156:159]
  s_waitcnt lgkmcnt(10)
  v_mfma_f32_16x16x32_bf16 v[72:75], v[188:191], v[172:175], v[72:75]
  ds_read_b64_tr_b16 v[204:205], v238 offset:2048
  ds_read_b64_tr_b16 v[206:207], v240 offset:2560
  s_waitcnt lgkmcnt(10)
  v_mfma_f32_16x16x32_bf16 v[76:79], v[192:195], v[172:175], v[76:79]
  ds_read_b128 v[168:171], v242 offset:32
  ds_read_b64_tr_b16 v[220:221], v232 offset:36864
  s_waitcnt lgkmcnt(10)
  v_mfma_f32_16x16x32_bf16 v[80:83], v[196:199], v[172:175], v[80:83]
  ds_read_b64_tr_b16 v[222:223], v233 offset:36864
  v_cvt_pk_bf16_f32 v160, v148, v149
  v_mfma_f32_16x16x32_bf16 v[120:123], v[172:175], v[200:203], v[120:123]
  v_cvt_pk_bf16_f32 v161, v150, v151
  v_cvt_pk_bf16_f32 v162, v152, v153
  s_waitcnt lgkmcnt(9)
  v_mfma_f32_16x16x32_bf16 v[84:87], v[188:191], v[176:179], v[84:87]
  v_cvt_pk_bf16_f32 v163, v154, v155
  v_cvt_pk_bf16_f32 v164, v156, v157
  v_mfma_f32_16x16x32_bf16 v[88:91], v[192:195], v[176:179], v[88:91]
  v_cvt_pk_bf16_f32 v165, v158, v159
  ds_write_b64 v245, v[160:161] offset:18432
  v_mfma_f32_16x16x32_bf16 v[92:95], v[196:199], v[176:179], v[92:95]
  ds_write_b64 v246, v[162:163] offset:18944
  ds_write_b64 v245, v[164:165] offset:19456
  s_waitcnt lgkmcnt(10)
  v_mfma_f32_16x16x32_bf16 v[96:99], v[188:191], v[180:183], v[96:99]
  ds_read_b64_tr_b16 v[224:225], v234 offset:36864
  ds_read_b64_tr_b16 v[226:227], v235 offset:36864
  v_mfma_f32_16x16x32_bf16 v[100:103], v[192:195], v[180:183], v[100:103]
  ds_read_b64_tr_b16 v[228:229], v236 offset:36864
  ds_read_b64_tr_b16 v[230:231], v237 offset:36864
  v_mfma_f32_16x16x32_bf16 v[104:107], v[196:199], v[180:183], v[104:107]
  ds_read_b64_tr_b16 v[208:209], v239 offset:2048
  ds_read_b64_tr_b16 v[210:211], v241 offset:2560
  s_waitcnt lgkmcnt(14)
  v_mfma_f32_16x16x32_bf16 v[108:111], v[188:191], v[184:187], v[108:111]
  ds_read_b64_tr_b16 v[212:213], v240 offset:2048
  ds_read_b64_tr_b16 v[214:215], v238 offset:2560
  v_mfma_f32_16x16x32_bf16 v[112:115], v[192:195], v[184:187], v[112:115]
  ds_read_b64_tr_b16 v[216:217], v241 offset:2048
  ds_read_b64_tr_b16 v[218:219], v239 offset:2560
  v_mfma_f32_16x16x32_bf16 v[116:119], v[196:199], v[184:187], v[116:119]
  s_cmp_lt_u32 s32, 10
  s_cbranch_scc1 .Lw81
  s_waitcnt vmcnt(6)
.Lw81:
  s_waitcnt vmcnt(8) lgkmcnt(0)
  s_barrier
  s_add_u32 s32, s32, 1
  s_cmp_eq_u32 s32, 12
  s_cbranch_scc1 .Lplast
  v_mfma_f32_16x16x32_bf16 v[120:123], v[204:207], v[168:171], v[120:123]
  v_mfma_f32_16x16x32_bf16 v[72:75], v[220:223], v[204:207], v[72:75]
  v_mfma_f32_16x16x32_bf16 v[76:79], v[224:227], v[204:207], v[76:79]
  s_mov_b32 m0, s92
  s_mov_b32 exec_hi, 0xffff
  global_load_lds_dwordx4 v253, s[30:31] offset:0 nt
  global_load_lds_dwordx4 v254, s[30:31] offset:768 nt
  s_mov_b32 exec_hi, -1
  s_add_u32 s30, s30, 0x1800
  s_addc_u32 s31, s31, 0x0
  v_mfma_f32_16x16x32_bf16 v[80:83], v[228:231], v[204:207], v[80:83]
  v_mfma_f32_16x16x32_bf16 v[84:87], v[220:223], v[208:211], v[84:87]
  v_mfma_f32_16x16x32_bf16 v[88:91], v[224:227], v[208:211], v[88:91]
  v_mfma_f32_16x16x32_bf16 v[92:95], v[228:231], v[208:211], v[92:95]
  s_add_u32 m0, s42, 12288
  s_nop 0
  global_load_lds_dwordx4 v247, s[28:29] offset:0 nt
  v_mfma_f32_16x16x32_bf16 v[96:99], v[220:223], v[212:215], v[96:99]
  ds_read_b32 v199, v242 offset:2052
  ds_read_b128 v[124:127], v243 offset:0
  ds_read_b128 v[128:131], v244 offset:0
  v_mfma_f32_16x16x32_bf16 v[100:103], v[224:227], v[212:215], v[100:103]
  ds_read_b128 v[132:135], v243 offset:128
  ds_read_b128 v[136:139], v244 offset:128
  ds_read_b128 v[140:143], v243 offset:256
  v_mfma_f32_16x16x32_bf16 v[104:107], v[228:231], v[212:215], v[104:107]
  ds_read_b128 v[144:147], v244 offset:256
  v_mfma_f32_16x16x32_bf16 v[108:111], v[220:223], v[216:219], v[108:111]
  global_load_lds_dwordx4 v248, s[28:29] offset:1024 nt
  v_mfma_f32_16x16x32_bf16 v[112:115], v[224:227], v[216:219], v[112:115]
  v_mfma_f32_16x16x32_bf16 v[116:119], v[228:231], v[216:219], v[116:119]
  s_waitcnt lgkmcnt(6)
  v_readfirstlane_b32 s45, v199
  s_cmp_eq_u32 s45, s32
  s_cbranch_scc1 .Lstub2
.Lcont2:
  s_waitcnt lgkmcnt(5)
  v_mfma_f32_16x16x32_bf16 v[148:151], v[124:127], v[0:3], 0
  ds_read_b128 v[200:203], v242 offset:256
  ds_read_b64_tr_b16 v[172:173], v238 offset:8192
  v_mfma_f32_16x16x32_bf16 v[152:155], v[124:127], v[24:27], 0
  global_load_lds_dwordx4 v249, s[28:29] offset:2048 nt
  ds_read_b64_tr_b16 v[174:175], v240 offset:8704
  ds_read_b64_tr_b16 v[188:189], v232 offset:18432
  v_mfma_f32_16x16x32_bf16 v[156:159], v[124:127], v[48:51], 0
  ds_read_b64_tr_b16 v[190:191], v233 offset:18432
  ds_read_b64_tr_b16 v[192:193], v234 offset:18432
  s_waitcnt lgkmcnt(10)
  v_mfma_f32_16x16x32_bf16 v[148:151], v[128:131], v[4:7], v[148:151]
  ds_read_b64_tr_b16 v[194:195], v235 offset:18432
  ds_read_b64_tr_b16 v[196:197], v236 offset:18432
  v_mfma_f32_16x16x32_bf16 v[152:155], v[128:131], v[28:31], v[152:155]
  ds_read_b64_tr_b16 v[198:199], v237 offset:18432
  ds_read_b64_tr_b16 v[176:177], v239 offset:8192
  v_mfma_f32_16x16x32_bf16 v[156:159], v[128:131], v[52:55], v[156:159]
  s_add_u32 m0, s42, 15360
  s_nop 0
  global_load_lds_dwordx4 v250, s[28:29] offset:0 nt
  ds_read_b64_tr_b16 v[178:179], v241 offset:8704
  ds_read_b64_tr_b16 v[180:181], v240 offset:8192
  s_waitcnt lgkmcnt(15)
  v_mfma_f32_16x16x32_bf16 v[148:151], v[132:135], v[8:11], v[148:151]
  ds_read_b64_tr_b16 v[182:183], v238 offset:8704
  ds_read_b64_tr_b16 v[184:185], v241 offset:8192
  v_mfma_f32_16x16x32_bf16 v[152:155], v[132:135], v[32:35], v[152:155]
  ds_read_b64_tr_b16 v[186:187], v239 offset:8704
  s_add_u32 s92, s92, 6144
  v_mfma_f32_16x16x32_bf16 v[156:159], v[132:135], v[56:59], v[156:159]
  s_cmp_eq_u32 s92, s46
  s_cselect_b32 s92, s43, s92
  s_movk_i32 s33, 6144
  s_waitcnt lgkmcnt(15)
  v_mfma_f32_16x16x32_bf16 v[148:151], v[136:139], v[12:15], v[148:151]
  global_load_lds_dwordx4 v251, s[28:29] offset:1024 nt
  s_cmp_eq_u32 s92, s55
  s_cmovk_i32 s33, -12288
  v_add_u32_e64 v243, s33, v243
  v_mfma_f32_16x16x32_bf16 v[152:155], v[136:139], v[36:39], v[152:155]
  v_add_u32_e64 v244, s33, v244
  v_mfma_f32_16x16x32_bf16 v[156:159], v[136:139], v[60:63], v[156:159]
  v_mfma_f32_16x16x32_bf16 v[148:151], v[140:143], v[16:19], v[148:151]
  v_mfma_f32_16x16x32_bf16 v[152:155], v[140:143], v[40:43], v[152:155]
  global_load_lds_dwordx4 v252, s[28:29] offset:2048 nt
  s_add_u32 s28, s28, 0x180
  s_addc_u32 s29, s29, 0x0
  v_mfma_f32_16x16x32_bf16 v[156:159], v[140:143], v[64:67], v[156:159]
  v_mfma_f32_16x16x32_bf16 v[148:151], v[144:147], v[20:23], v[148:151]
  v_mfma_f32_16x16x32_bf16 v[152:155], v[144:147], v[44:47], v[152:155]
  v_mfma_f32_16x16x32_bf16 v[156:159], v[144:147], v[68:71], v[156:159]
  s_waitcnt lgkmcnt(10)
  v_mfma_f32_16x16x32_bf16 v[72:75], v[188:191], v[172:175], v[72:75]
  ds_read_b64_tr_b16 v[204:205], v238 offset:10240
  ds_read_b64_tr_b16 v[206:207], v240 offset:10752
  s_waitcnt lgkmcnt(10)
  v_mfma_f32_16x16x32_bf16 v[76:79], v[192:195], v[172:175], v[76:79]
  ds_read_b128 v[168:171], v242 offset:288
  ds_read_b64_tr_b16 v[220:221], v232 offset:43008
  s_waitcnt lgkmcnt(10)
  v_mfma_f32_16x16x32_bf16 v[80:83], v[196:199], v[172:175], v[80:83]
  ds_read_b64_tr_b16 v[222:223], v233 offset:43008
  v_cvt_pk_bf16_f32 v160, v148, v149
  v_mfma_f32_16x16x32_bf16 v[120:123], v[172:175], v[200:203], v[120:123]
  v_cvt_pk_bf16_f32 v161, v150, v151
  v_cvt_pk_bf16_f32 v162, v152, v153
  s_waitcnt lgkmcnt(9)
  v_mfma_f32_16x16x32_bf16 v[84:87], v[188:191], v[176:179], v[84:87]
  v_cvt_pk_bf16_f32 v163, v154, v155
  v_cvt_pk_bf16_f32 v164, v156, v157
  v_mfma_f32_16x16x32_bf16 v[88:91], v[192:195], v[176:179], v[88:91]
  v_cvt_pk_bf16_f32 v165, v158, v159
  ds_read_b64_tr_b16 v[224:225], v234 offset:43008
  v_mfma_f32_16x16x32_bf16 v[92:95], v[196:199], v[176:179], v[92:95]
  ds_read_b64_tr_b16 v[226:227], v235 offset:43008
  ds_read_b64_tr_b16 v[228:229], v236 offset:43008
  s_waitcnt lgkmcnt(10)
  v_mfma_f32_16x16x32_bf16 v[96:99], v[188:191], v[180:183], v[96:99]
  ds_read_b64_tr_b16 v[230:231], v237 offset:43008
  ds_read_b64_tr_b16 v[208:209], v239 offset:10240
  v_mfma_f32_16x16x32_bf16 v[100:103], v[192:195], v[180:183], v[100:103]
  ds_read_b64_tr_b16 v[210:211], v241 offset:10752
  ds_read_b64_tr_b16 v[212:213], v240 offset:10240
  v_mfma_f32_16x16x32_bf16 v[104:107], v[196:199], v[180:183], v[104:107]
  ds_read_b64_tr_b16 v[214:215], v238 offset:10752
  ds_read_b64_tr_b16 v[216:217], v241 offset:10240
  s_waitcnt lgkmcnt(14)
  v_mfma_f32_16x16x32_bf16 v[108:111], v[188:191], v[184:187], v[108:111]
  ds_read_b64_tr_b16 v[218:219], v239 offset:10752
  v_mfma_f32_16x16x32_bf16 v[112:115], v[192:195], v[184:187], v[112:115]
  v_mfma_f32_16x16x32_bf16 v[116:119], v[196:199], v[184:187], v[116:119]
  s_waitcnt vmcnt(8) lgkmcnt(0)
  s_barrier
  s_add_u32 s32, s32, 1
  v_mfma_f32_16x16x32_bf16 v[120:123], v[204:207], v[168:171], v[120:123]
  v_mfma_f32_16x16x32_bf16 v[72:75], v[220:223], v[204:207], v[72:75]
  v_mfma_f32_16x16x32_bf16 v[76:79], v[224:227], v[204:207], v[76:79]
  s_mov_b32 m0, s92
  s_mov_b32 exec_hi, 0xffff
  global_load_lds_dwordx4 v253, s[30:31] offset:0 nt
  global_load_lds_dwordx4 v254, s[30:31] offset:768 nt
  s_mov_b32 exec_hi, -1
  s_add_u32 s30, s30, 0x1800
  s_addc_u32 s31, s31, 0x0
  v_mfma_f32_16x16x32_bf16 v[80:83], v[228:231], v[204:207], v[80:83]
  v_mfma_f32_16x16x32_bf16 v[84:87], v[220:223], v[208:211], v[84:87]
  v_mfma_f32_16x16x32_bf16 v[88:91], v[224:227], v[208:211], v[88:91]
  v_mfma_f32_16x16x32_bf16 v[92:95], v[228:231], v[208:211], v[92:95]
  s_add_u32 m0, s42, 18432
  s_nop 0
  global_load_lds_dwordx4 v247, s[28:29] offset:0 nt
  v_mfma_f32_16x16x32_bf16 v[96:99], v[220:223], v[212:215], v[96:99]
  ds_read_b32 v199, v242 offset:2048
  ds_read_b128 v[124:127], v243 offset:0
  ds_read_b128 v[128:131], v244 offset:0
  v_mfma_f32_16x16x32_bf16 v[100:103], v[224:227], v[212:215], v[100:103]
  ds_read_b128 v[132:135], v243 offset:128
  ds_read_b128 v[136:139], v244 offset:128
  ds_read_b128 v[140:143], v243 offset:256
  v_mfma_f32_16x16x32_bf16 v[104:107], v[228:231], v[212:215], v[104:107]
  ds_read_b128 v[144:147], v244 offset:256
  ds_write_b64 v245, v[160:161] offset:0
  ds_write_b64 v246, v[162:163] offset:512
  v_mfma_f32_16x16x32_bf16 v[108:111], v[220:223], v[216:219], v[108:111]
  global_load_lds_dwordx4 v248, s[28:29] offset:1024 nt
  ds_write_b64 v245, v[164:165] offset:1024
  v_mfma_f32_16x16x32_bf16 v[112:115], v[224:227], v[216:219], v[112:115]
  v_mfma_f32_16x16x32_bf16 v[116:119], v[228:231], v[216:219], v[116:119]
  s_waitcnt lgkmcnt(9)
  v_readfirstlane_b32 s45, v199
  s_cmp_eq_u32 s45, s32
  s_cbranch_scc1 .Lstub3
.Lcont3:
  s_waitcnt lgkmcnt(8)
  v_mfma_f32_16x16x32_bf16 v[148:151], v[124:127], v[0:3], 0
  ds_read_b128 v[200:203], v242 offset:0
  ds_read_b64_tr_b16 v[172:173], v238 offset:0
  v_mfma_f32_16x16x32_bf16 v[152:155], v[124:127], v[24:27], 0
  global_load_lds_dwordx4 v249, s[28:29] offset:2048 nt
  ds_read_b64_tr_b16 v[174:175], v240 offset:512
  ds_read_b64_tr_b16 v[188:189], v232 offset:0
  v_mfma_f32_16x16x32_bf16 v[156:159], v[124:127], v[48:51], 0
  ds_read_b64_tr_b16 v[190:191], v233 offset:0
  ds_read_b64_tr_b16 v[192:193], v234 offset:0
  s_waitcnt lgkmcnt(13)
  v_mfma_f32_16x16x32_bf16 v[148:151], v[128:131], v[4:7], v[148:151]
  ds_read_b64_tr_b16 v[194:195], v235 offset:0
  ds_read_b64_tr_b16 v[196:197], v236 offset:0
  v_mfma_f32_16x16x32_bf16 v[152:155], v[128:131], v[28:31], v[152:155]
  ds_read_b64_tr_b16 v[198:199], v237 offset:0
  ds_read_b64_tr_b16 v[176:177], v239 offset:0
  v_mfma_f32_16x16x32_bf16 v[156:159], v[128:131], v[52:55], v[156:159]
  s_add_u32 m0, s42, 21504
  s_nop 0
  global_load_lds_dwordx4 v250, s[28:29] offset:0 nt
  ds_read_b64_tr_b16 v[178:179], v241 offset:512
  ds_read_b64_tr_b16 v[180:181], v240 offset:0
  s_waitcnt lgkmcnt(15)
  v_mfma_f32_16x16x32_bf16 v[148:151], v[132:135], v[8:11], v[148:151]
  ds_read_b64_tr_b16 v[182:183], v238 offset:512
  ds_read_b64_tr_b16 v[184:185], v241 offset:0
  v_mfma_f32_16x16x32_bf16 v[152:155], v[132:135], v[32:35], v[152:155]
  ds_read_b64_tr_b16 v[186:187], v239 offset:512
  s_add_u32 s92, s92, 6144
  v_mfma_f32_16x16x32_bf16 v[156:159], v[132:135], v[56:59], v[156:159]
  s_cmp_eq_u32 s92, s46
  s_cselect_b32 s92, s43, s92
  s_movk_i32 s33, 6144
  v_mfma_f32_16x16x32_bf16 v[148:151], v[136:139], v[12:15], v[148:151]
  global_load_lds_dwordx4 v251, s[28:29] offset:1024 nt
  s_cmp_eq_u32 s92, s55
  s_cmovk_i32 s33, -12288
  v_add_u32 v243, s33, v243
  v_mfma_f32_16x16x32_bf16 v[152:155], v[136:139], v[36:39], v[152:155]
  v_add_u32 v244, s33, v244
  v_mfma_f32_16x16x32_bf16 v[156:159], v[136:139], v[60:63], v[156:159]
  v_mfma_f32_16x16x32_bf16 v[148:151], v[140:143], v[16:19], v[148:151]
  v_mfma_f32_16x16x32_bf16 v[152:155], v[140:143], v[40:43], v[152:155]
  global_load_lds_dwordx4 v252, s[28:29] offset:2048 nt
  s_add_u32 s28, s28, 0xfffdbe80
  s_addc_u32 s29, s29, -1
  v_mfma_f32_16x16x32_bf16 v[156:159], v[140:143], v[64:67], v[156:159]
  v_mfma_f32_16x16x32_bf16 v[148:151], v[144:147], v[20:23], v[148:151]
  v_mfma_f32_16x16x32_bf16 v[152:155], v[144:147], v[44:47], v[152:155]
  v_mfma_f32_16x16x32_bf16 v[156:159], v[144:147], v[68:71], v[156:159]
  s_waitcnt lgkmcnt(10)
  v_mfma_f32_16x16x32_bf16 v[72:75], v[188:191], v[172:175], v[72:75]
  ds_read_b64_tr_b16 v[204:205], v238 offset:2048
  ds_read_b64_tr_b16 v[206:207], v240 offset:2560
  s_waitcnt lgkmcnt(10)
  v_mfma_f32_16x16x32_bf16 v[76:79], v[192:195], v[172:175], v[76:79]
  ds_read_b128 v[168:171], v242 offset:32
  ds_read_b64_tr_b16 v[220:221], v232 offset:24576
  s_waitcnt lgkmcnt(10)
  v_mfma_f32_16x16x32_bf16 v[80:83], v[196:199], v[172:175], v[80:83]
  ds_read_b64_tr_b16 v[222:223], v233 offset:24576
  v_cvt_pk_bf16_f32 v160, v148, v149
  v_mfma_f32_16x16x32_bf16 v[120:123], v[172:175], v[200:203], v[120:123]
  v_cvt_pk_bf16_f32 v161, v150, v151
  v_cvt_pk_bf16_f32 v162, v152, v153
  s_waitcnt lgkmcnt(9)
  v_mfma_f32_16x16x32_bf16 v[84:87], v[188:191], v[176:179], v[84:87]
  v_cvt_pk_bf16_f32 v163, v154, v155
  v_cvt_pk_bf16_f32 v164, v156, v157
  v_mfma_f32_16x16x32_bf16 v[88:91], v[192:195], v[176:179], v[88:91]
  v_cvt_pk_bf16_f32 v165, v158, v159
  ds_write_b64 v245, v[160:161] offset:6144
  v_mfma_f32_16x16x32_bf16 v[92:95], v[196:199], v[176:179], v[92:95]
  ds_write_b64 v246, v[162:163] offset:6656
  ds_write_b64 v245, v[164:165] offset:7168
  s_waitcnt lgkmcnt(10)
  v_mfma_f32_16x16x32_bf16 v[96:99], v[188:191], v[180:183], v[96:99]
  ds_read_b64_tr_b16 v[224:225], v234 offset:24576
  ds_read_b64_tr_b16 v[226:227], v235 offset:24576
  v_mfma_f32_16x16x32_bf16 v[100:103], v[192:195], v[180:183], v[100:103]
  ds_read_b64_tr_b16 v[228:229], v236 offset:24576
  ds_read_b64_tr_b16 v[230:231], v237 offset:24576
  v_mfma_f32_16x16x32_bf16 v[104:107], v[196:199], v[180:183], v[104:107]
  ds_read_b64_tr_b16 v[208:209], v239 offset:2048
  ds_read_b64_tr_b16 v[210:211], v241 offset:2560
  s_waitcnt lgkmcnt(14)
  v_mfma_f32_16x16x32_bf16 v[108:111], v[188:191], v[184:187], v[108:111]
  ds_read_b64_tr_b16 v[212:213], v240 offset:2048
  ds_read_b64_tr_b16 v[214:215], v238 offset:2560
  v_mfma_f32_16x16x32_bf16 v[112:115], v[192:195], v[184:187], v[112:115]
  ds_read_b64_tr_b16 v[216:217], v241 offset:2048
  ds_read_b64_tr_b16 v[218:219], v239 offset:2560
  v_mfma_f32_16x16x32_bf16 v[116:119], v[196:199], v[184:187], v[116:119]
  s_waitcnt vmcnt(8) lgkmcnt(0)
  s_barrier
  s_add_u32 s32, s32, 1
  s_branch .Lploop
.Lplast:
  v_mfma_f32_16x16x32_bf16 v[120:123], v[204:207], v[168:171], v[120:123]
  v_mbcnt_lo_u32_b32 v253, -1, 0
  v_mbcnt_hi_u32_b32 v253, -1, v253
  v_lshlrev_b32_e64 v156, 4, v253
  v_mfma_f32_16x16x32_bf16 v[72:75], v[220:223], v[204:207], v[72:75]
  s_add_u32 m0, s42, 12288
  s_nop 0
  global_load_lds_dwordx4 v247, s[28:29] offset:0 nt
  v_lshrrev_b32 v254, 4, v253
  v_bfe_u32 v255, v253, 2, 2
  v_lshl_add_u32 v254, v254, 3, v255
  v_mfma_f32_16x16x32_bf16 v[76:79], v[224:227], v[204:207], v[76:79]
  v_mul_u32_u24 v254, 96, v254
  v_and_b32_e64 v255, 3, v253
  v_lshl_add_u32 v254, v255, 3, v254
  v_mfma_f32_16x16x32_bf16 v[80:83], v[228:231], v[204:207], v[80:83]
  global_load_lds_dwordx4 v248, s[28:29] offset:1024 nt
  s_mul_i32 s92, s27, 3072
  s_add_u32 s92, s92, 114688
  s_mul_i32 s46, s27, 6144
  v_mfma_f32_16x16x32_bf16 v[84:87], v[220:223], v[208:211], v[84:87]
  s_add_u32 s46, s46, 133120
  v_add_u32 v157, s92, v254
  v_add_u32 v158, s46, v254
  v_mfma_f32_16x16x32_bf16 v[88:91], v[224:227], v[208:211], v[88:91]
  v_add_u32 v159, s42, v254
  s_mul_i32 s33, s27, 18432
  s_add_u32 s16, s16, s33
  s_addc_u32 s17, s17, 0
  v_mfma_f32_16x16x32_bf16 v[92:95], v[228:231], v[208:211], v[92:95]
  global_load_lds_dwordx4 v249, s[28:29] offset:2048 nt
  v_mfma_f32_16x16x32_bf16 v[96:99], v[220:223], v[212:215], v[96:99]
  ds_read_b32 v160, v242 offset:2052
  ds_read_b128 v[200:203], v242 offset:256
  ds_read_b64_tr_b16 v[172:173], v238 offset:8192
  v_mfma_f32_16x16x32_bf16 v[100:103], v[224:227], v[212:215], v[100:103]
  s_add_u32 m0, s42, 15360
  s_nop 0
  global_load_lds_dwordx4 v250, s[28:29] offset:0 nt
  ds_read_b64_tr_b16 v[174:175], v240 offset:8704
  ds_read_b64_tr_b16 v[188:189], v232 offset:18432
  ds_read_b64_tr_b16 v[190:191], v233 offset:18432
  v_mfma_f32_16x16x32_bf16 v[104:107], v[228:231], v[212:215], v[104:107]
  ds_read_b64_tr_b16 v[192:193], v234 offset:18432
  ds_read_b64_tr_b16 v[194:195], v235 offset:18432
  ds_read_b64_tr_b16 v[196:197], v236 offset:18432
  v_mfma_f32_16x16x32_bf16 v[108:111], v[220:223], v[216:219], v[108:111]
  ds_read_b64_tr_b16 v[198:199], v237 offset:18432
  ds_read_b64_tr_b16 v[176:177], v239 offset:8192
  ds_read_b64_tr_b16 v[178:179], v241 offset:8704
  v_mfma_f32_16x16x32_bf16 v[112:115], v[224:227], v[216:219], v[112:115]
  global_load_lds_dwordx4 v251, s[28:29] offset:1024 nt
  ds_read_b64_tr_b16 v[180:181], v240 offset:8192
  ds_read_b64_tr_b16 v[182:183], v238 offset:8704
  ds_read_b64_tr_b16 v[184:185], v241 offset:8192
  v_mfma_f32_16x16x32_bf16 v[116:119], v[228:231], v[216:219], v[116:119]
  ds_read_b64_tr_b16 v[186:187], v239 offset:8704
  s_waitcnt lgkmcnt(15)
  v_readfirstlane_b32 s45, v160
  s_cmp_eq_u32 s45, s32
  s_cbranch_scc1 .Lstub4
.Lcont4:
  s_waitcnt lgkmcnt(6)
  v_mfma_f32_16x16x32_bf16 v[72:75], v[188:191], v[172:175], v[72:75]
  global_load_lds_dwordx4 v252, s[28:29] offset:2048 nt
  s_add_u32 s28, s28, 0x180
  s_addc_u32 s29, s29, 0x0
  ds_read_b64_tr_b16 v[204:205], v238 offset:10240
  ds_read_b64_tr_b16 v[206:207], v240 offset:10752
  s_waitcnt lgkmcnt(10)
  v_mfma_f32_16x16x32_bf16 v[76:79], v[192:195], v[172:175], v[76:79]
  ds_read_b128 v[168:171], v242 offset:288
  ds_read_b64_tr_b16 v[220:221], v232 offset:43008
  s_waitcnt lgkmcnt(10)
  v_mfma_f32_16x16x32_bf16 v[80:83], v[196:199], v[172:175], v[80:83]
  ds_read_b64_tr_b16 v[222:223], v233 offset:43008
  ds_read_b64_tr_b16 v[224:225], v234 offset:43008
  v_mfma_f32_16x16x32_bf16 v[120:123], v[172:175], v[200:203], v[120:123]
  s_add_u32 m0, s92, 0
  s_nop 0
  global_load_lds_dwordx4 v156, s[16:17] offset:0
  ds_read_b64_tr_b16 v[226:227], v235 offset:43008
  ds_read_b64_tr_b16 v[228:229], v236 offset:43008
  s_waitcnt lgkmcnt(12)
  v_mfma_f32_16x16x32_bf16 v[84:87], v[188:191], v[176:179], v[84:87]
  ds_read_b64_tr_b16 v[230:231], v237 offset:43008
  ds_read_b64_tr_b16 v[208:209], v239 offset:10240
  v_mfma_f32_16x16x32_bf16 v[88:91], v[192:195], v[176:179], v[88:91]
  global_load_lds_dwordx4 v156, s[16:17] offset:1024
  ds_read_b64_tr_b16 v[210:211], v241 offset:10752
  ds_read_b64_tr_b16 v[212:213], v240 offset:10240
  v_mfma_f32_16x16x32_bf16 v[92:95], v[196:199], v[176:179], v[92:95]
  ds_read_b64_tr_b16 v[214:215], v238 offset:10752
  ds_read_b64_tr_b16 v[216:217], v241 offset:10240
  s_waitcnt lgkmcnt(15)
  v_mfma_f32_16x16x32_bf16 v[96:99], v[188:191], v[180:183], v[96:99]
  ds_read_b64_tr_b16 v[218:219], v239 offset:10752
  v_mfma_f32_16x16x32_bf16 v[100:103], v[192:195], v[180:183], v[100:103]
  global_load_lds_dwordx4 v156, s[16:17] offset:2048
  s_add_u32 s16, s16, 0xc00
  s_addc_u32 s17, s17, 0x0
  v_mfma_f32_16x16x32_bf16 v[104:107], v[196:199], v[180:183], v[104:107]
  s_waitcnt lgkmcnt(15)
  v_mfma_f32_16x16x32_bf16 v[108:111], v[188:191], v[184:187], v[108:111]
  v_mfma_f32_16x16x32_bf16 v[112:115], v[192:195], v[184:187], v[112:115]
  v_mfma_f32_16x16x32_bf16 v[116:119], v[196:199], v[184:187], v[116:119]
  s_waitcnt vmcnt(9) lgkmcnt(0)
  s_barrier
  s_add_u32 s32, s32, 1
  v_mfma_f32_16x16x32_bf16 v[120:123], v[204:207], v[168:171], v[120:123]
  v_mbcnt_lo_u32_b32 v246, -1, 0
  v_mbcnt_hi_u32_b32 v246, -1, v246
  v_lshrrev_b32 v245, 4, v246
  v_mfma_f32_16x16x32_bf16 v[72:75], v[220:223], v[204:207], v[72:75]
  s_add_u32 m0, s42, 18432
  s_nop 0
  global_load_lds_dwordx4 v247, s[28:29] offset:0 nt
  v_and_b32 v246, 15, v246
  s_mov_b32 s33, 0x75643120
  v_and_b32_e64 v161, 14, v246
  v_mfma_f32_16x16x32_bf16 v[76:79], v[224:227], v[204:207], v[76:79]
  v_lshlrev_b32_e64 v161, 1, v161
  v_bfe_u32 v161, s33, v161, 3
  v_mul_u32_u24 v162, 384, v246
  v_mfma_f32_16x16x32_bf16 v[80:83], v[228:231], v[204:207], v[80:83]
  global_load_lds_dwordx4 v248, s[28:29] offset:1024 nt
  v_and_b32_e64 v163, 1, v245
  v_lshl_add_u32 v162, v163, 3, v162
  v_lshrrev_b32 v163, 1, v245
  v_mfma_f32_16x16x32_bf16 v[84:87], v[220:223], v[208:211], v[84:87]
  s_mul_i32 s33, s27, 6
  v_add3_u32 v164, v163, s33, 0
  v_xor_b32_e64 v164, v164, v161
  v_mfma_f32_16x16x32_bf16 v[88:91], v[224:227], v[208:211], v[88:91]
  global_load_lds_dwordx4 v249, s[28:29] offset:2048 nt
  v_lshl_add_u32 v145, v164, 4, v162
  v_add3_u32 v164, v163, s33, 2
  v_xor_b32_e64 v164, v164, v161
  v_mfma_f32_16x16x32_bf16 v[92:95], v[228:231], v[208:211], v[92:95]
  v_lshl_add_u32 v146, v164, 4, v162
  v_add3_u32 v164, v163, s33, 4
  v_xor_b32 v164, v164, v161
  v_mfma_f32_16x16x32_bf16 v[96:99], v[220:223], v[212:215], v[96:99]
  s_add_u32 m0, s42, 21504
  s_nop 0
  global_load_lds_dwordx4 v250, s[28:29] offset:0 nt
  v_lshl_add_u32 v147, v164, 4, v162
  ds_read_b32 v160, v242 offset:2048
  ds_read_b128 v[200:203], v242 offset:0
  v_mfma_f32_16x16x32_bf16 v[100:103], v[224:227], v[212:215], v[100:103]
  ds_read_b64_tr_b16 v[172:173], v238 offset:0
  ds_read_b64_tr_b16 v[174:175], v240 offset:512
  ds_read_b64_tr_b16 v[188:189], v232 offset:0
  v_mfma_f32_16x16x32_bf16 v[104:107], v[228:231], v[212:215], v[104:107]
  global_load_lds_dwordx4 v251, s[28:29] offset:1024 nt
  ds_read_b64_tr_b16 v[190:191], v233 offset:0
  ds_read_b64_tr_b16 v[192:193], v234 offset:0
  ds_read_b64_tr_b16 v[194:195], v235 offset:0
  v_mfma_f32_16x16x32_bf16 v[108:111], v[220:223], v[216:219], v[108:111]
  ds_read_b64_tr_b16 v[196:197], v236 offset:0
  ds_read_b64_tr_b16 v[198:199], v237 offset:0
  ds_read_b64_tr_b16 v[176:177], v239 offset:0
  v_mfma_f32_16x16x32_bf16 v[112:115], v[224:227], v[216:219], v[112:115]
  global_load_lds_dwordx4 v252, s[28:29] offset:2048 nt
  ds_read_b64_tr_b16 v[178:179], v241 offset:512
  ds_read_b64_tr_b16 v[180:181], v240 offset:0
  ds_read_b64_tr_b16 v[182:183], v238 offset:512
  v_mfma_f32_16x16x32_bf16 v[116:119], v[228:231], v[216:219], v[116:119]
  ds_read_b64_tr_b16 v[184:185], v241 offset:0
  ds_read_b64_tr_b16 v[186:187], v239 offset:512
  s_waitcnt lgkmcnt(15)
  v_readfirstlane_b32 s45, v160
  s_cmp_eq_u32 s45, s32
  s_cbranch_scc1 .Lstub5
.Lcont5:
  s_waitcnt lgkmcnt(6)
  v_mfma_f32_16x16x32_bf16 v[72:75], v[188:191], v[172:175], v[72:75]
  s_add_u32 m0, s46, 0
  s_nop 0
  global_load_lds_dwordx4 v156, s[16:17] offset:0
  ds_read_b64_tr_b16 v[204:205], v238 offset:2048
  ds_read_b64_tr_b16 v[206:207], v240 offset:2560
  s_waitcnt lgkmcnt(10)
  v_mfma_f32_16x16x32_bf16 v[76:79], v[192:195], v[172:175], v[76:79]
  ds_read_b128 v[168:171], v242 offset:32
  ds_read_b64_tr_b16 v[220:221], v232 offset:24576
  s_waitcnt lgkmcnt(10)
  v_mfma_f32_16x16x32_bf16 v[80:83], v[196:199], v[172:175], v[80:83]
  global_load_lds_dwordx4 v156, s[16:17] offset:1024
  ds_read_b64_tr_b16 v[222:223], v233 offset:24576
  ds_read_b64_tr_b16 v[224:225], v234 offset:24576
  v_mfma_f32_16x16x32_bf16 v[120:123], v[172:175], v[200:203], v[120:123]
  ds_read_b64_tr_b16 v[226:227], v235 offset:24576
  ds_read_b64_tr_b16 v[228:229], v236 offset:24576
  s_waitcnt lgkmcnt(12)
  v_mfma_f32_16x16x32_bf16 v[84:87], v[188:191], v[176:179], v[84:87]
  global_load_lds_dwordx4 v156, s[16:17] offset:2048
  s_add_u32 s16, s16, 0xc00
  s_addc_u32 s17, s17, 0x0
  ds_read_b64_tr_b16 v[230:231], v237 offset:24576
  ds_read_b64_tr_b16 v[208:209], v239 offset:2048
  v_mfma_f32_16x16x32_bf16 v[88:91], v[192:195], v[176:179], v[88:91]
  ds_read_b64_tr_b16 v[210:211], v241 offset:2560
  ds_read_b64_tr_b16 v[212:213], v240 offset:2048
  v_mfma_f32_16x16x32_bf16 v[92:95], v[196:199], v[176:179], v[92:95]
  ds_read_b64_tr_b16 v[214:215], v238 offset:2560
  ds_read_b64_tr_b16 v[216:217], v241 offset:2048
  s_waitcnt lgkmcnt(15)
  v_mfma_f32_16x16x32_bf16 v[96:99], v[188:191], v[180:183], v[96:99]
  ds_read_b64_tr_b16 v[218:219], v239 offset:2560
  v_mfma_f32_16x16x32_bf16 v[100:103], v[192:195], v[180:183], v[100:103]
  v_mfma_f32_16x16x32_bf16 v[104:107], v[196:199], v[180:183], v[104:107]
  s_waitcnt lgkmcnt(15)
  v_mfma_f32_16x16x32_bf16 v[108:111], v[188:191], v[184:187], v[108:111]
  v_mfma_f32_16x16x32_bf16 v[112:115], v[192:195], v[184:187], v[112:115]
  v_mfma_f32_16x16x32_bf16 v[116:119], v[196:199], v[184:187], v[116:119]
  s_waitcnt vmcnt(9)
  ds_read_b64_tr_b16 v[0:1], v157 offset:0
  ds_read_b64_tr_b16 v[2:3], v157 offset:384
  ds_read_b64_tr_b16 v[24:25], v157 offset:32
  ds_read_b64_tr_b16 v[26:27], v157 offset:416
  ds_read_b64_tr_b16 v[48:49], v157 offset:64
  ds_read_b64_tr_b16 v[50:51], v157 offset:448
  s_waitcnt vmcnt(9) lgkmcnt(0)
  s_barrier
  s_add_u32 s32, s32, 1
  v_mfma_f32_16x16x32_bf16 v[120:123], v[204:207], v[168:171], v[120:123]
  s_xor_b32 s33, s27, 0
  s_mul_i32 s33, s33, 24576
  v_add_u32_e64 v136, s33, v145
  v_mfma_f32_16x16x32_bf16 v[72:75], v[220:223], v[204:207], v[72:75]
  v_add_u32_e64 v137, s33, v146
  v_add_u32 v138, s33, v147
  s_xor_b32 s33, s27, 1
  v_mfma_f32_16x16x32_bf16 v[76:79], v[224:227], v[204:207], v[76:79]
  s_mul_i32 s33, s33, 24576
  v_add_u32 v139, s33, v145
  v_add_u32 v140, s33, v146
  v_mfma_f32_16x16x32_bf16 v[80:83], v[228:231], v[204:207], v[80:83]
  v_add_u32 v141, s33, v147
  s_xor_b32 s33, s27, 2
  s_mul_i32 s33, s33, 24576
  v_mfma_f32_16x16x32_bf16 v[84:87], v[220:223], v[208:211], v[84:87]
  v_add_u32 v142, s33, v145
  v_add_u32 v143, s33, v146
  v_add_u32 v144, s33, v147
  v_mfma_f32_16x16x32_bf16 v[88:91], v[224:227], v[208:211], v[88:91]
  s_xor_b32 s33, s27, 3
  s_mul_i32 s33, s33, 24576
  v_add_u32_e64 v145, s33, v145
  v_mfma_f32_16x16x32_bf16 v[92:95], v[228:231], v[208:211], v[92:95]
  v_add_u32 v146, s33, v146
  v_add_u32 v147, s33, v147
  v_mfma_f32_16x16x32_bf16 v[96:99], v[220:223], v[212:215], v[96:99]
  ds_read_b32 v160, v242 offset:2052
  ds_read_b128 v[200:203], v242 offset:256
  ds_read_b64_tr_b16 v[172:173], v238 offset:8192
  v_mfma_f32_16x16x32_bf16 v[100:103], v[224:227], v[212:215], v[100:103]
  ds_read_b64_tr_b16 v[174:175], v240 offset:8704
  ds_read_b64_tr_b16 v[188:189], v232 offset:6144
  ds_read_b64_tr_b16 v[190:191], v233 offset:6144
  v_mfma_f32_16x16x32_bf16 v[104:107], v[228:231], v[212:215], v[104:107]
  ds_read_b64_tr_b16 v[192:193], v234 offset:6144
  ds_read_b64_tr_b16 v[194:195], v235 offset:6144
  ds_read_b64_tr_b16 v[196:197], v236 offset:6144
  v_mfma_f32_16x16x32_bf16 v[108:111], v[220:223], v[216:219], v[108:111]
  ds_read_b64_tr_b16 v[198:199], v237 offset:6144
  ds_read_b64_tr_b16 v[176:177], v239 offset:8192
  ds_read_b64_tr_b16 v[178:179], v241 offset:8704
  v_mfma_f32_16x16x32_bf16 v[112:115], v[224:227], v[216:219], v[112:115]
  ds_read_b64_tr_b16 v[180:181], v240 offset:8192
  ds_read_b64_tr_b16 v[182:183], v238 offset:8704
  ds_read_b64_tr_b16 v[184:185], v241 offset:8192
  v_mfma_f32_16x16x32_bf16 v[116:119], v[228:231], v[216:219], v[116:119]
  ds_read_b64_tr_b16 v[186:187], v239 offset:8704
  s_waitcnt lgkmcnt(15)
  v_readfirstlane_b32 s45, v160
  s_cmp_eq_u32 s45, s32
  s_cbranch_scc1 .Lstub6
.Lcont6:
  s_waitcnt lgkmcnt(6)
  v_mfma_f32_16x16x32_bf16 v[72:75], v[188:191], v[172:175], v[72:75]
  ds_read_b64_tr_b16 v[204:205], v238 offset:10240
  ds_read_b64_tr_b16 v[206:207], v240 offset:10752
  s_waitcnt lgkmcnt(10)
  v_mfma_f32_16x16x32_bf16 v[76:79], v[192:195], v[172:175], v[76:79]
  ds_read_b128 v[168:171], v242 offset:288
  ds_read_b64_tr_b16 v[220:221], v232 offset:30720
  s_waitcnt lgkmcnt(10)
  v_mfma_f32_16x16x32_bf16 v[80:83], v[196:199], v[172:175], v[80:83]
  s_add_u32 m0, s46, 3072
  s_nop 0
  global_load_lds_dwordx4 v156, s[16:17] offset:0
  ds_read_b64_tr_b16 v[222:223], v233 offset:30720
  ds_read_b64_tr_b16 v[224:225], v234 offset:30720
  v_mfma_f32_16x16x32_bf16 v[120:123], v[172:175], v[200:203], v[120:123]
  global_load_lds_dwordx4 v156, s[16:17] offset:1024
  ds_read_b64_tr_b16 v[226:227], v235 offset:30720
  ds_read_b64_tr_b16 v[228:229], v236 offset:30720
  s_waitcnt lgkmcnt(12)
  v_mfma_f32_16x16x32_bf16 v[84:87], v[188:191], v[176:179], v[84:87]
  ds_read_b64_tr_b16 v[230:231], v237 offset:30720
  ds_read_b64_tr_b16 v[208:209], v239 offset:10240
  v_mfma_f32_16x16x32_bf16 v[88:91], v[192:195], v[176:179], v[88:91]
  global_load_lds_dwordx4 v156, s[16:17] offset:2048
  s_add_u32 s16, s16, 0xc00
  s_addc_u32 s17, s17, 0x0
  ds_read_b64_tr_b16 v[210:211], v241 offset:10752
  ds_read_b64_tr_b16 v[212:213], v240 offset:10240
  v_mfma_f32_16x16x32_bf16 v[92:95], v[196:199], v[176:179], v[92:95]
  s_add_u32 m0, s92, 0
  s_nop 0
  global_load_lds_dwordx4 v156, s[16:17] offset:0
  ds_read_b64_tr_b16 v[214:215], v238 offset:10752
  ds_read_b64_tr_b16 v[216:217], v241 offset:10240
  s_waitcnt lgkmcnt(15)
  v_mfma_f32_16x16x32_bf16 v[96:99], v[188:191], v[180:183], v[96:99]
  ds_read_b64_tr_b16 v[218:219], v239 offset:10752
  v_mov_b32 v252, 0x3f803f80
  v_mfma_f32_16x16x32_bf16 v[100:103], v[192:195], v[180:183], v[100:103]
  global_load_lds_dwordx4 v156, s[16:17] offset:1024
  v_mov_b32 v253, 0x3f803f80
  v_mov_b32 v254, 0x3f803f80
  v_mfma_f32_16x16x32_bf16 v[104:107], v[196:199], v[180:183], v[104:107]
  global_load_lds_dwordx4 v156, s[16:17] offset:2048
  s_add_u32 s16, s16, 0xc00
  s_addc_u32 s17, s17, 0x0
  v_mov_b32 v255, 0x3f803f80
  v_xor_b32 v164, v245, v161
  s_waitcnt lgkmcnt(15)
  v_mfma_f32_16x16x32_bf16 v[108:111], v[188:191], v[184:187], v[108:111]
  v_mul_u32_u24 v243, 384, v246
  v_lshl_add_u32 v243, v164, 4, v243
  v_mfma_f32_16x16x32_bf16 v[112:115], v[192:195], v[184:187], v[112:115]
  s_add_u32 m0, s42, 0
  s_nop 0
  global_load_lds_dwordx4 v156, s[16:17] offset:0
  v_add_u32 v243, s42, v243
  v_xor_b32 v244, 64, v243
  v_mfma_f32_16x16x32_bf16 v[116:119], v[196:199], v[184:187], v[116:119]
  global_load_lds_dwordx4 v156, s[16:17] offset:1024
  v_and_b32 v161, 1, v246
  v_mul_u32_u24 v161, 0x202, v161
  s_waitcnt lgkmcnt(12)
  v_mfma_f32_16x16x32_bf16 v[120:123], v[204:207], v[168:171], v[120:123]
  v_lshlrev_b32 v161, 16, v161
  v_sub_u32 v165, 0x07060100, v161
  s_waitcnt lgkmcnt(10)
  v_mfma_f32_16x16x32_bf16 v[72:75], v[220:223], v[204:207], v[72:75]
  global_load_lds_dwordx4 v156, s[16:17] offset:2048
  s_add_u32 s16, s16, 0xc00
  s_addc_u32 s17, s17, 0x0
  v_lshrrev_b32 v161, 2, v246
  v_and_b32_e64 v162, 3, v246
  s_waitcnt lgkmcnt(8)
  v_mfma_f32_16x16x32_bf16 v[76:79], v[224:227], v[204:207], v[76:79]
  s_add_u32 m0, s42, 3072
  s_nop 0
  global_load_lds_dwordx4 v156, s[16:17] offset:0
  v_lshl_add_u32 v161, v245, 4, v161
  v_lshlrev_b32 v161, 14, v161
  s_waitcnt lgkmcnt(6)
  v_mfma_f32_16x16x32_bf16 v[80:83], v[228:231], v[204:207], v[80:83]
  v_lshl_add_u32 v161, v162, 4, v161
  s_lshl_b32 s33, s27, 20
  s_waitcnt lgkmcnt(4)
  v_mfma_f32_16x16x32_bf16 v[84:87], v[220:223], v[208:211], v[84:87]
  global_load_lds_dwordx4 v156, s[16:17] offset:1024
  s_lshl_b32 s34, s2, 6
  s_add_u32 s33, s33, s34
  v_mfma_f32_16x16x32_bf16 v[88:91], v[224:227], v[208:211], v[88:91]
  global_load_lds_dwordx4 v156, s[16:17] offset:2048
  s_add_u32 s33, s33, 65536
  v_lshlrev_b32_e64 v246, 4, v245
  v_mfma_f32_16x16x32_bf16 v[92:95], v[228:231], v[208:211], v[92:95]
  v_add_u32_e64 v245, s33, v161
  s_lshl_b32 s33, s27, 6
  s_waitcnt lgkmcnt(2)
  v_mfma_f32_16x16x32_bf16 v[96:99], v[220:223], v[212:215], v[96:99]
  s_add_u32 s33, s33, 159232
  v_add_u32 v246, s33, v246
  v_mfma_f32_16x16x32_bf16 v[100:103], v[224:227], v[212:215], v[100:103]
  s_waitcnt vmcnt(12)
  ds_read_b64_tr_b16 v[4:5], v158 offset:0
  ds_read_b64_tr_b16 v[6:7], v158 offset:384
  ds_read_b64_tr_b16 v[28:29], v158 offset:32
  ds_read_b64_tr_b16 v[30:31], v158 offset:416
  ds_read_b64_tr_b16 v[52:53], v158 offset:64
  ds_read_b64_tr_b16 v[54:55], v158 offset:448
  v_mfma_f32_16x16x32_bf16 v[104:107], v[228:231], v[212:215], v[104:107]
  s_waitcnt lgkmcnt(6)
  v_mfma_f32_16x16x32_bf16 v[108:111], v[220:223], v[216:219], v[108:111]
  v_mfma_f32_16x16x32_bf16 v[112:115], v[224:227], v[216:219], v[112:115]
  v_mfma_f32_16x16x32_bf16 v[116:119], v[228:231], v[216:219], v[116:119]
  s_mov_b32 s50, 0xcccccccc
  s_mov_b32 s51, 0xcccccccc
  s_waitcnt vmcnt(12) lgkmcnt(0)
  s_barrier
  s_add_u32 s32, s32, 1
  v_mfma_f32_16x16x32_bf16 v[124:127], v[252:255], v[0:3], 0
  v_mfma_f32_16x16x32_bf16 v[128:131], v[252:255], v[24:27], 0
  v_mfma_f32_16x16x32_bf16 v[132:135], v[252:255], v[48:51], 0
  ds_read_b32 v204, v242 offset:2048
  ds_read_b128 v[200:203], v242 offset:0
  ds_read_b64_tr_b16 v[172:173], v238 offset:0
  v_mfma_f32_16x16x32_bf16 v[124:127], v[252:255], v[4:7], v[124:127]
  ds_read_b64_tr_b16 v[174:175], v240 offset:512
  ds_read_b64_tr_b16 v[188:189], v232 offset:12288
  ds_read_b64_tr_b16 v[190:191], v233 offset:12288
  v_mfma_f32_16x16x32_bf16 v[128:131], v[252:255], v[28:31], v[128:131]
  ds_read_b64_tr_b16 v[192:193], v234 offset:12288
  ds_read_b64_tr_b16 v[194:195], v235 offset:12288
  ds_read_b64_tr_b16 v[196:197], v236 offset:12288
  v_mfma_f32_16x16x32_bf16 v[132:135], v[252:255], v[52:55], v[132:135]
  ds_read_b64_tr_b16 v[198:199], v237 offset:12288
  ds_read_b64_tr_b16 v[176:177], v239 offset:0
  ds_read_b64_tr_b16 v[178:179], v241 offset:512
  ds_read_b64_tr_b16 v[180:181], v240 offset:0
  ds_read_b64_tr_b16 v[182:183], v238 offset:512
  ds_read_b64_tr_b16 v[184:185], v241 offset:0
  ds_read_b64_tr_b16 v[186:187], v239 offset:512
  s_waitcnt lgkmcnt(15)
  v_readfirstlane_b32 s45, v204
  s_cmp_eq_u32 s45, s32
  s_cbranch_scc1 .Lstub7
.Lcont7:
  s_waitcnt lgkmcnt(6)
  v_mfma_f32_16x16x32_bf16 v[72:75], v[188:191], v[172:175], v[72:75]
  ds_read_b64_tr_b16 v[204:205], v238 offset:2048
  ds_read_b64_tr_b16 v[206:207], v240 offset:2560
  s_waitcnt lgkmcnt(10)
  v_mfma_f32_16x16x32_bf16 v[76:79], v[192:195], v[172:175], v[76:79]
  ds_read_b128 v[248:251], v242 offset:32
  ds_read_b64_tr_b16 v[220:221], v232 offset:36864
  s_waitcnt lgkmcnt(10)
  v_mfma_f32_16x16x32_bf16 v[80:83], v[196:199], v[172:175], v[80:83]
  ds_read_b64_tr_b16 v[222:223], v233 offset:36864
  ds_read_b64_tr_b16 v[224:225], v234 offset:36864
  v_mfma_f32_16x16x32_bf16 v[120:123], v[172:175], v[200:203], v[120:123]
  ds_read_b64_tr_b16 v[226:227], v235 offset:36864
  ds_read_b64_tr_b16 v[228:229], v236 offset:36864
  s_waitcnt lgkmcnt(12)
  v_mfma_f32_16x16x32_bf16 v[84:87], v[188:191], v[176:179], v[84:87]
  ds_read_b64_tr_b16 v[230:231], v237 offset:36864
  ds_read_b64_tr_b16 v[208:209], v239 offset:2048
  v_mfma_f32_16x16x32_bf16 v[88:91], v[192:195], v[176:179], v[88:91]
  ds_read_b64_tr_b16 v[210:211], v241 offset:2560
  ds_read_b64_tr_b16 v[212:213], v240 offset:2048
  v_mfma_f32_16x16x32_bf16 v[92:95], v[196:199], v[176:179], v[92:95]
  ds_read_b64_tr_b16 v[214:215], v238 offset:2560
  ds_read_b64_tr_b16 v[216:217], v241 offset:2048
  s_waitcnt lgkmcnt(15)
  v_mfma_f32_16x16x32_bf16 v[96:99], v[188:191], v[180:183], v[96:99]
  ds_read_b64_tr_b16 v[218:219], v239 offset:2560
  s_waitcnt vmcnt(0)
  v_mfma_f32_16x16x32_bf16 v[100:103], v[192:195], v[180:183], v[100:103]
  ds_read_b64_tr_b16 v[8:9], v158 offset:3072
  ds_read_b64_tr_b16 v[10:11], v158 offset:3456
  v_mfma_f32_16x16x32_bf16 v[104:107], v[196:199], v[180:183], v[104:107]
  ds_read_b64_tr_b16 v[32:33], v158 offset:3104
  ds_read_b64_tr_b16 v[34:35], v158 offset:3488
  s_waitcnt lgkmcnt(15)
  v_mfma_f32_16x16x32_bf16 v[108:111], v[188:191], v[184:187], v[108:111]
  ds_read_b64_tr_b16 v[56:57], v158 offset:3136
  ds_read_b64_tr_b16 v[58:59], v158 offset:3520
  v_mfma_f32_16x16x32_bf16 v[112:115], v[192:195], v[184:187], v[112:115]
  ds_read_b64_tr_b16 v[12:13], v157 offset:0
  ds_read_b64_tr_b16 v[14:15], v157 offset:384
  v_mfma_f32_16x16x32_bf16 v[116:119], v[196:199], v[184:187], v[116:119]
  ds_read_b64_tr_b16 v[36:37], v157 offset:32
  ds_read_b64_tr_b16 v[38:39], v157 offset:416
  v_mfma_f32_16x16x32_bf16 v[120:123], v[204:207], v[248:251], v[120:123]
  ds_read_b64_tr_b16 v[60:61], v157 offset:64
  ds_read_b64_tr_b16 v[62:63], v157 offset:448
  s_waitcnt lgkmcnt(15)
  v_mfma_f32_16x16x32_bf16 v[72:75], v[220:223], v[204:207], v[72:75]
  ds_read_b64_tr_b16 v[16:17], v159 offset:0
  ds_read_b64_tr_b16 v[18:19], v159 offset:384
  v_mfma_f32_16x16x32_bf16 v[76:79], v[224:227], v[204:207], v[76:79]
  ds_read_b64_tr_b16 v[40:41], v159 offset:32
  ds_read_b64_tr_b16 v[42:43], v159 offset:416
  v_mfma_f32_16x16x32_bf16 v[80:83], v[228:231], v[204:207], v[80:83]
  ds_read_b64_tr_b16 v[64:65], v159 offset:64
  ds_read_b64_tr_b16 v[66:67], v159 offset:448
  v_mfma_f32_16x16x32_bf16 v[84:87], v[220:223], v[208:211], v[84:87]
  ds_read_b64_tr_b16 v[20:21], v159 offset:3072
  ds_read_b64_tr_b16 v[22:23], v159 offset:3456
  v_mfma_f32_16x16x32_bf16 v[88:91], v[224:227], v[208:211], v[88:91]
  ds_read_b64_tr_b16 v[44:45], v159 offset:3104
  ds_read_b64_tr_b16 v[46:47], v159 offset:3488
  v_mfma_f32_16x16x32_bf16 v[92:95], v[228:231], v[208:211], v[92:95]
  ds_read_b64_tr_b16 v[68:69], v159 offset:3136
  ds_read_b64_tr_b16 v[70:71], v159 offset:3520
  s_waitcnt lgkmcnt(15)
  v_mfma_f32_16x16x32_bf16 v[96:99], v[220:223], v[212:215], v[96:99]
  v_mfma_f32_16x16x32_bf16 v[100:103], v[224:227], v[212:215], v[100:103]
  v_mfma_f32_16x16x32_bf16 v[104:107], v[228:231], v[212:215], v[104:107]
  v_mfma_f32_16x16x32_bf16 v[108:111], v[220:223], v[216:219], v[108:111]
  v_mfma_f32_16x16x32_bf16 v[112:115], v[224:227], v[216:219], v[112:115]
  v_mfma_f32_16x16x32_bf16 v[116:119], v[228:231], v[216:219], v[116:119]
  v_mfma_f32_16x16x32_bf16 v[124:127], v[252:255], v[8:11], v[124:127]
  v_mfma_f32_16x16x32_bf16 v[128:131], v[252:255], v[32:35], v[128:131]
  v_mfma_f32_16x16x32_bf16 v[132:135], v[252:255], v[56:59], v[132:135]
  v_mfma_f32_16x16x32_bf16 v[124:127], v[252:255], v[12:15], v[124:127]
  s_waitcnt lgkmcnt(14)
  v_mfma_f32_16x16x32_bf16 v[128:131], v[252:255], v[36:39], v[128:131]
  s_waitcnt lgkmcnt(12)
  v_mfma_f32_16x16x32_bf16 v[132:135], v[252:255], v[60:63], v[132:135]
  s_waitcnt lgkmcnt(10)
  v_mfma_f32_16x16x32_bf16 v[124:127], v[252:255], v[16:19], v[124:127]
  s_waitcnt lgkmcnt(8)
  v_mfma_f32_16x16x32_bf16 v[128:131], v[252:255], v[40:43], v[128:131]
  s_waitcnt lgkmcnt(6)
  v_mfma_f32_16x16x32_bf16 v[132:135], v[252:255], v[64:67], v[132:135]
  s_waitcnt lgkmcnt(4)
  v_mfma_f32_16x16x32_bf16 v[124:127], v[252:255], v[20:23], v[124:127]
  s_waitcnt lgkmcnt(2)
  v_mfma_f32_16x16x32_bf16 v[128:131], v[252:255], v[44:47], v[128:131]
  s_waitcnt lgkmcnt(0)
  v_mfma_f32_16x16x32_bf16 v[132:135], v[252:255], v[68:71], v[132:135]
  s_waitcnt lgkmcnt(0)
  s_barrier
  s_add_u32 s32, s32, 1
  ds_read_b32 v204, v242 offset:2052
  ds_read_b128 v[200:203], v242 offset:256
  ds_read_b64_tr_b16 v[172:173], v238 offset:8192
  ds_read_b64_tr_b16 v[174:175], v240 offset:8704
  ds_read_b64_tr_b16 v[188:189], v232 offset:18432
  ds_read_b64_tr_b16 v[190:191], v233 offset:18432
  ds_read_b64_tr_b16 v[192:193], v234 offset:18432
  ds_read_b64_tr_b16 v[194:195], v235 offset:18432
  ds_read_b64_tr_b16 v[196:197], v236 offset:18432
  ds_read_b64_tr_b16 v[198:199], v237 offset:18432
  ds_read_b64_tr_b16 v[176:177], v239 offset:8192
  ds_read_b64_tr_b16 v[178:179], v241 offset:8704
  ds_read_b64_tr_b16 v[180:181], v240 offset:8192
  ds_read_b64_tr_b16 v[182:183], v238 offset:8704
  ds_read_b64_tr_b16 v[184:185], v241 offset:8192
  ds_read_b64_tr_b16 v[186:187], v239 offset:8704
  s_waitcnt lgkmcnt(15)
  v_readfirstlane_b32 s45, v204
  s_cmp_eq_u32 s45, s32
  s_cbranch_scc1 .Lstub8
.Lcont8:
  s_waitcnt lgkmcnt(6)
  v_mfma_f32_16x16x32_bf16 v[72:75], v[188:191], v[172:175], v[72:75]
  ds_read_b64_tr_b16 v[204:205], v238 offset:10240
  ds_read_b64_tr_b16 v[206:207], v240 offset:10752
  s_waitcnt lgkmcnt(10)
  v_mfma_f32_16x16x32_bf16 v[76:79], v[192:195], v[172:175], v[76:79]
  ds_read_b128 v[248:251], v242 offset:288
  ds_read_b64_tr_b16 v[220:221], v232 offset:43008
  s_waitcnt lgkmcnt(10)
  v_mfma_f32_16x16x32_bf16 v[80:83], v[196:199], v[172:175], v[80:83]
  ds_read_b64_tr_b16 v[222:223], v233 offset:43008
  ds_read_b64_tr_b16 v[224:225], v234 offset:43008
  v_mfma_f32_16x16x32_bf16 v[120:123], v[172:175], v[200:203], v[120:123]
  ds_read_b64_tr_b16 v[226:227], v235 offset:43008
  ds_read_b64_tr_b16 v[228:229], v236 offset:43008
  s_waitcnt lgkmcnt(12)
  v_mfma_f32_16x16x32_bf16 v[84:87], v[188:191], v[176:179], v[84:87]
  ds_read_b64_tr_b16 v[230:231], v237 offset:43008
  ds_read_b64_tr_b16 v[208:209], v239 offset:10240
  v_mfma_f32_16x16x32_bf16 v[88:91], v[192:195], v[176:179], v[88:91]
  ds_read_b64_tr_b16 v[210:211], v241 offset:10752
  ds_read_b64_tr_b16 v[212:213], v240 offset:10240
  v_mfma_f32_16x16x32_bf16 v[92:95], v[196:199], v[176:179], v[92:95]
  ds_read_b64_tr_b16 v[214:215], v238 offset:10752
  ds_read_b64_tr_b16 v[216:217], v241 offset:10240
  s_waitcnt lgkmcnt(15)
  v_mfma_f32_16x16x32_bf16 v[96:99], v[188:191], v[180:183], v[96:99]
  ds_read_b64_tr_b16 v[218:219], v239 offset:10752
  ds_read_b128 v[148:151], v246 offset:0
  v_mfma_f32_16x16x32_bf16 v[100:103], v[192:195], v[180:183], v[100:103]
  v_mfma_f32_16x16x32_bf16 v[104:107], v[196:199], v[180:183], v[104:107]
  s_waitcnt lgkmcnt(15)
  v_mfma_f32_16x16x32_bf16 v[108:111], v[188:191], v[184:187], v[108:111]
  v_mfma_f32_16x16x32_bf16 v[112:115], v[192:195], v[184:187], v[112:115]
  v_mfma_f32_16x16x32_bf16 v[116:119], v[196:199], v[184:187], v[116:119]
  s_waitcnt lgkmcnt(13)
  v_mfma_f32_16x16x32_bf16 v[120:123], v[204:207], v[248:251], v[120:123]
  s_waitcnt lgkmcnt(11)
  v_mfma_f32_16x16x32_bf16 v[72:75], v[220:223], v[204:207], v[72:75]
  s_waitcnt lgkmcnt(9)
  v_mfma_f32_16x16x32_bf16 v[76:79], v[224:227], v[204:207], v[76:79]
  s_waitcnt lgkmcnt(7)
  v_mfma_f32_16x16x32_bf16 v[80:83], v[228:231], v[204:207], v[80:83]
  s_waitcnt lgkmcnt(5)
  v_mfma_f32_16x16x32_bf16 v[84:87], v[220:223], v[208:211], v[84:87]
  v_add_f32_dpp v152, v120, v120 quad_perm:[1,0,3,2] row_mask:0xf bank_mask:0xf
  v_add_f32_dpp v153, v121, v121 quad_perm:[1,0,3,2] row_mask:0xf bank_mask:0xf
  v_mfma_f32_16x16x32_bf16 v[88:91], v[224:227], v[208:211], v[88:91]
  v_add_f32_dpp v154, v122, v122 quad_perm:[1,0,3,2] row_mask:0xf bank_mask:0xf
  v_add_f32_dpp v155, v123, v123 quad_perm:[1,0,3,2] row_mask:0xf bank_mask:0xf
  v_mfma_f32_16x16x32_bf16 v[92:95], v[228:231], v[208:211], v[92:95]
  v_cvt_pk_bf16_f32 v172, v72, v73
  v_cvt_pk_bf16_f32 v173, v74, v75
  s_waitcnt lgkmcnt(3)
  v_mfma_f32_16x16x32_bf16 v[96:99], v[220:223], v[212:215], v[96:99]
  ds_write_b64 v136, v[172:173]
  v_cvt_pk_bf16_f32 v174, v76, v77
  v_mfma_f32_16x16x32_bf16 v[100:103], v[224:227], v[212:215], v[100:103]
  v_cvt_pk_bf16_f32 v175, v78, v79
  ds_write_b64 v137, v[174:175]
  v_mfma_f32_16x16x32_bf16 v[104:107], v[228:231], v[212:215], v[104:107]
  v_cvt_pk_bf16_f32 v176, v80, v81
  v_cvt_pk_bf16_f32 v177, v82, v83
  s_waitcnt lgkmcnt(3)
  v_mfma_f32_16x16x32_bf16 v[108:111], v[220:223], v[216:219], v[108:111]
  ds_write_b64 v138, v[176:177]
  v_cvt_pk_bf16_f32 v178, v84, v85
  v_mfma_f32_16x16x32_bf16 v[112:115], v[224:227], v[216:219], v[112:115]
  v_cvt_pk_bf16_f32 v179, v86, v87
  ds_write_b64 v139, v[178:179]
  v_mfma_f32_16x16x32_bf16 v[116:119], v[228:231], v[216:219], v[116:119]
  v_cvt_pk_bf16_f32 v180, v88, v89
  v_cvt_pk_bf16_f32 v181, v90, v91
  ds_write_b64 v140, v[180:181]
  v_cvt_pk_bf16_f32 v182, v92, v93
  v_cvt_pk_bf16_f32 v183, v94, v95
  ds_write_b64 v141, v[182:183]
  v_cvt_pk_bf16_f32 v172, v96, v97
  v_cvt_pk_bf16_f32 v173, v98, v99
  ds_write_b64 v142, v[172:173]
  v_cvt_pk_bf16_f32 v174, v100, v101
  v_cvt_pk_bf16_f32 v175, v102, v103
  ds_write_b64 v143, v[174:175]
  v_cvt_pk_bf16_f32 v176, v104, v105
  v_cvt_pk_bf16_f32 v177, v106, v107
  ds_write_b64 v144, v[176:177]
  v_cvt_pk_bf16_f32 v178, v108, v109
  v_cvt_pk_bf16_f32 v179, v110, v111
  ds_write_b64 v145, v[178:179]
  v_cvt_pk_bf16_f32 v180, v112, v113
  v_cvt_pk_bf16_f32 v181, v114, v115
  ds_write_b64 v146, v[180:181]
  v_cvt_pk_bf16_f32 v182, v116, v117
  v_cvt_pk_bf16_f32 v183, v118, v119
  ds_write_b64 v147, v[182:183]
  s_waitcnt lgkmcnt(12)
  v_cndmask_b32 v152, v152, v148, s[50:51]
  v_cndmask_b32 v153, v153, v149, s[50:51]
  v_cndmask_b32 v154, v154, v150, s[50:51]
  v_cndmask_b32 v155, v155, v151, s[50:51]
  s_waitcnt lgkmcnt(0)
  s_barrier
  ds_read_b128 v[172:175], v243 offset:0
  ds_read_b128 v[176:179], v244 offset:0
  ds_read_b128 v[180:183], v243 offset:128
  ds_read_b128 v[184:187], v244 offset:128
  ds_read_b128 v[188:191], v243 offset:256
  ds_read_b128 v[192:195], v244 offset:256
  v_mov_b32 v225, s54
  v_mov_b32 v227, s54
  v_mov_b32 v229, s54
  v_mov_b32 v231, s54
  v_mov_b32 v137, s54
  v_mov_b32 v139, s54
  v_mov_b32 v141, s54
  v_mov_b32 v143, s54
  s_waitcnt lgkmcnt(5)
  v_mfma_f32_16x16x32_bf16 v[196:199], v[172:175], v[252:255], 0
  v_mfma_f32_16x16x32_bf16 v[204:207], v[172:175], v[0:3], 0
  v_mfma_f32_16x16x32_bf16 v[208:211], v[172:175], v[24:27], 0
  v_mfma_f32_16x16x32_bf16 v[212:215], v[172:175], v[48:51], 0
  s_waitcnt lgkmcnt(4)
  v_mfma_f32_16x16x32_bf16 v[196:199], v[176:179], v[252:255], v[196:199]
  v_mfma_f32_16x16x32_bf16 v[204:207], v[176:179], v[4:7], v[204:207]
  v_mfma_f32_16x16x32_bf16 v[208:211], v[176:179], v[28:31], v[208:211]
  v_mfma_f32_16x16x32_bf16 v[212:215], v[176:179], v[52:55], v[212:215]
  s_waitcnt lgkmcnt(3)
  v_mfma_f32_16x16x32_bf16 v[196:199], v[180:183], v[252:255], v[196:199]
  v_mfma_f32_16x16x32_bf16 v[204:207], v[180:183], v[8:11], v[204:207]
  v_mfma_f32_16x16x32_bf16 v[208:211], v[180:183], v[32:35], v[208:211]
  v_mfma_f32_16x16x32_bf16 v[212:215], v[180:183], v[56:59], v[212:215]
  s_waitcnt lgkmcnt(2)
  v_mfma_f32_16x16x32_bf16 v[196:199], v[184:187], v[252:255], v[196:199]
  v_mfma_f32_16x16x32_bf16 v[204:207], v[184:187], v[12:15], v[204:207]
  v_mfma_f32_16x16x32_bf16 v[208:211], v[184:187], v[36:39], v[208:211]
  v_mfma_f32_16x16x32_bf16 v[212:215], v[184:187], v[60:63], v[212:215]
  s_waitcnt lgkmcnt(1)
  v_mfma_f32_16x16x32_bf16 v[196:199], v[188:191], v[252:255], v[196:199]
  v_mfma_f32_16x16x32_bf16 v[204:207], v[188:191], v[16:19], v[204:207]
  v_mfma_f32_16x16x32_bf16 v[208:211], v[188:191], v[40:43], v[208:211]
  v_mfma_f32_16x16x32_bf16 v[212:215], v[188:191], v[64:67], v[212:215]
  s_waitcnt lgkmcnt(0)
  v_mfma_f32_16x16x32_bf16 v[196:199], v[192:195], v[252:255], v[196:199]
  v_mfma_f32_16x16x32_bf16 v[204:207], v[192:195], v[20:23], v[204:207]
  v_mfma_f32_16x16x32_bf16 v[208:211], v[192:195], v[44:47], v[208:211]
  v_mfma_f32_16x16x32_bf16 v[212:215], v[192:195], v[68:71], v[212:215]
  s_nop 0
  s_nop 3
  v_mul_f32 v196, 0xbbaaaaab, v196
  v_mul_f32 v197, 0xbbaaaaab, v197
  v_mul_f32 v198, 0xbbaaaaab, v198
  v_mul_f32 v199, 0xbbaaaaab, v199
  v_fmac_f32 v204, v196, v124
  v_fmac_f32 v208, v196, v128
  v_fmac_f32_e64 v212, v196, v132
  v_cvt_pk_bf16_f32 v224, v204, v208
  v_cvt_pk_bf16_f32 v226, v212, v212
  v_perm_b32 v226, v152, v226, v165
  global_store_dwordx4 v245, v[224:227], s[24:25] sc1
  v_add_u32 v245, 0x10000, v245
  v_fmac_f32 v205, v197, v124
  v_fmac_f32 v209, v197, v128
  v_fmac_f32_e64 v213, v197, v132
  v_cvt_pk_bf16_f32 v228, v205, v209
  v_cvt_pk_bf16_f32 v230, v213, v213
  v_perm_b32 v230, v153, v230, v165
  global_store_dwordx4 v245, v[228:231], s[24:25] sc1
  v_add_u32 v245, 0x10000, v245
  v_fmac_f32 v206, v198, v124
  v_fmac_f32 v210, v198, v128
  v_fmac_f32_e64 v214, v198, v132
  v_cvt_pk_bf16_f32 v136, v206, v210
  v_cvt_pk_bf16_f32 v138, v214, v214
  v_perm_b32 v138, v154, v138, v165
  global_store_dwordx4 v245, v[136:139], s[24:25] sc1
  v_add_u32 v245, 0x10000, v245
  v_fmac_f32 v207, v199, v124
  v_fmac_f32 v211, v199, v128
  v_fmac_f32_e64 v215, v199, v132
  v_cvt_pk_bf16_f32 v140, v207, v211
  v_cvt_pk_bf16_f32 v142, v215, v215
  v_perm_b32 v142, v155, v142, v165
  global_store_dwordx4 v245, v[140:143], s[24:25] sc1
  s_barrier
  s_endpgm
.Lstub0:
  s_movk_i32 s47, 0
  s_branch .Lpslow
.Lstub1:
  s_movk_i32 s47, 1
  s_branch .Lpslow
.Lstub2:
  s_movk_i32 s47, 2
  s_branch .Lpslow
.Lstub3:
  s_movk_i32 s47, 3
  s_branch .Lpslow
.Lstub4:
  s_movk_i32 s47, 4
  s_branch .Lpslow
.Lstub5:
  s_movk_i32 s47, 5
  s_branch .Lpslow
.Lstub6:
  s_movk_i32 s47, 6
  s_branch .Lpslow
.Lstub7:
  s_movk_i32 s47, 7
  s_branch .Lpslow
.Lstub8:
  s_movk_i32 s47, 8
  s_branch .Lpslow
.Lpslow:
  s_waitcnt vmcnt(0) lgkmcnt(0)
  s_barrier
  s_barrier
  v_mbcnt_lo_u32_b32 v205, -1, 0
  v_mbcnt_hi_u32_b32 v205, -1, v205
  v_lshrrev_b32 v204, 4, v205
  v_and_b32 v205, 15, v205
  v_lshlrev_b32 v204, 4, v204
  v_lshlrev_b32 v205, 2, v205
  s_lshl_b32 s33, s27, 6
  s_add_u32 s33, s33, 159488
  v_add_u32_e64 v204, s33, v204
  ds_read_b128 v[212:215], v204 offset:0
  s_xor_b32 s33, s27, 0
  s_lshl_b32 s33, s33, 6
  s_add_u32 s33, s33, 159488
  v_add_u32_e64 v208, s33, v205
  ds_read_b32 v208, v208
  s_xor_b32 s33, s27, 1
  s_lshl_b32 s33, s33, 6
  s_add_u32 s33, s33, 159488
  v_add_u32_e64 v209, s33, v205
  ds_read_b32 v209, v209
  s_xor_b32 s33, s27, 2
  s_lshl_b32 s33, s33, 6
  s_add_u32 s33, s33, 159488
  v_add_u32_e64 v210, s33, v205
  ds_read_b32 v210, v210
  s_xor_b32 s33, s27, 3
  s_lshl_b32 s33, s33, 6
  s_add_u32 s33, s33, 159488
  v_add_u32_e64 v211, s33, v205
  ds_read_b32 v211, v211
  s_waitcnt lgkmcnt(3)
  v_cvt_i32_f32 v208, -v208
  s_waitcnt lgkmcnt(2)
  v_cvt_i32_f32 v209, -v209
  s_waitcnt lgkmcnt(1)
  v_cvt_i32_f32 v210, -v210
  s_waitcnt lgkmcnt(0)
  v_cvt_i32_f32 v211, -v211
  v_cvt_i32_f32 v212, -v212
  v_cvt_i32_f32 v213, -v213
  v_cvt_i32_f32 v214, -v214
  v_cvt_i32_f32 v215, -v215
  v_ldexp_f32 v72, v72, v208
  v_ldexp_f32 v73, v73, v208
  v_ldexp_f32 v74, v74, v208
  v_ldexp_f32 v75, v75, v208
  v_ldexp_f32 v76, v76, v208
  v_ldexp_f32 v77, v77, v208
  v_ldexp_f32 v78, v78, v208
  v_ldexp_f32 v79, v79, v208
  v_ldexp_f32 v80, v80, v208
  v_ldexp_f32 v81, v81, v208
  v_ldexp_f32 v82, v82, v208
  v_ldexp_f32 v83, v83, v208
  v_ldexp_f32 v84, v84, v209
  v_ldexp_f32 v85, v85, v209
  v_ldexp_f32 v86, v86, v209
  v_ldexp_f32 v87, v87, v209
  v_ldexp_f32 v88, v88, v209
  v_ldexp_f32 v89, v89, v209
  v_ldexp_f32 v90, v90, v209
  v_ldexp_f32 v91, v91, v209
  v_ldexp_f32 v92, v92, v209
  v_ldexp_f32 v93, v93, v209
  v_ldexp_f32 v94, v94, v209
  v_ldexp_f32 v95, v95, v209
  v_ldexp_f32 v96, v96, v210
  v_ldexp_f32 v97, v97, v210
  v_ldexp_f32 v98, v98, v210
  v_ldexp_f32 v99, v99, v210
  v_ldexp_f32 v100, v100, v210
  v_ldexp_f32 v101, v101, v210
  v_ldexp_f32 v102, v102, v210
  v_ldexp_f32 v103, v103, v210
  v_ldexp_f32 v104, v104, v210
  v_ldexp_f32 v105, v105, v210
  v_ldexp_f32 v106, v106, v210
  v_ldexp_f32 v107, v107, v210
  v_ldexp_f32 v108, v108, v211
  v_ldexp_f32 v109, v109, v211
  v_ldexp_f32 v110, v110, v211
  v_ldexp_f32 v111, v111, v211
  v_ldexp_f32 v112, v112, v211
  v_ldexp_f32 v113, v113, v211
  v_ldexp_f32 v114, v114, v211
  v_ldexp_f32 v115, v115, v211
  v_ldexp_f32 v116, v116, v211
  v_ldexp_f32 v117, v117, v211
  v_ldexp_f32 v118, v118, v211
  v_ldexp_f32 v119, v119, v211
  v_ldexp_f32 v120, v120, v212
  v_ldexp_f32 v121, v121, v213
  v_ldexp_f32 v122, v122, v214
  v_ldexp_f32 v123, v123, v215
  s_cmp_eq_u32 s47, 0
  s_cbranch_scc1 .Lback0
  s_cmp_eq_u32 s47, 1
  s_cbranch_scc1 .Lback1
  s_cmp_eq_u32 s47, 2
  s_cbranch_scc1 .Lback2
  s_cmp_eq_u32 s47, 3
  s_cbranch_scc1 .Lback3
  s_cmp_eq_u32 s47, 4
  s_cbranch_scc1 .Lback4
  s_cmp_eq_u32 s47, 5
  s_cbranch_scc1 .Lback5
  s_cmp_eq_u32 s47, 6
  s_cbranch_scc1 .Lback6
  s_cmp_eq_u32 s47, 7
  s_cbranch_scc1 .Lback7
.Lback8:
  ds_read_b64_tr_b16 v[172:173], v238 offset:8192
  ds_read_b64_tr_b16 v[174:175], v240 offset:8704
  ds_read_b64_tr_b16 v[176:177], v239 offset:8192
  ds_read_b64_tr_b16 v[178:179], v241 offset:8704
  ds_read_b64_tr_b16 v[180:181], v240 offset:8192
  ds_read_b64_tr_b16 v[182:183], v238 offset:8704
  ds_read_b64_tr_b16 v[184:185], v241 offset:8192
  ds_read_b64_tr_b16 v[186:187], v239 offset:8704
  s_branch .Lcont8
.Lback7:
  ds_read_b64_tr_b16 v[172:173], v238 offset:0
  ds_read_b64_tr_b16 v[174:175], v240 offset:512
  ds_read_b64_tr_b16 v[176:177], v239 offset:0
  ds_read_b64_tr_b16 v[178:179], v241 offset:512
  ds_read_b64_tr_b16 v[180:181], v240 offset:0
  ds_read_b64_tr_b16 v[182:183], v238 offset:512
  ds_read_b64_tr_b16 v[184:185], v241 offset:0
  ds_read_b64_tr_b16 v[186:187], v239 offset:512
  s_branch .Lcont7
.Lback6:
  ds_read_b64_tr_b16 v[172:173], v238 offset:8192
  ds_read_b64_tr_b16 v[174:175], v240 offset:8704
  ds_read_b64_tr_b16 v[176:177], v239 offset:8192
  ds_read_b64_tr_b16 v[178:179], v241 offset:8704
  ds_read_b64_tr_b16 v[180:181], v240 offset:8192
  ds_read_b64_tr_b16 v[182:183], v238 offset:8704
  ds_read_b64_tr_b16 v[184:185], v241 offset:8192
  ds_read_b64_tr_b16 v[186:187], v239 offset:8704
  s_branch .Lcont6
.Lback5:
  ds_read_b64_tr_b16 v[172:173], v238 offset:0
  ds_read_b64_tr_b16 v[174:175], v240 offset:512
  ds_read_b64_tr_b16 v[176:177], v239 offset:0
  ds_read_b64_tr_b16 v[178:179], v241 offset:512
  ds_read_b64_tr_b16 v[180:181], v240 offset:0
  ds_read_b64_tr_b16 v[182:183], v238 offset:512
  ds_read_b64_tr_b16 v[184:185], v241 offset:0
  ds_read_b64_tr_b16 v[186:187], v239 offset:512
  s_branch .Lcont5
.Lback4:
  ds_read_b64_tr_b16 v[172:173], v238 offset:8192
  ds_read_b64_tr_b16 v[174:175], v240 offset:8704
  ds_read_b64_tr_b16 v[176:177], v239 offset:8192
  ds_read_b64_tr_b16 v[178:179], v241 offset:8704
  ds_read_b64_tr_b16 v[180:181], v240 offset:8192
  ds_read_b64_tr_b16 v[182:183], v238 offset:8704
  ds_read_b64_tr_b16 v[184:185], v241 offset:8192
  ds_read_b64_tr_b16 v[186:187], v239 offset:8704
  s_branch .Lcont4
.Lback3:
  s_branch .Lcont3
.Lback2:
  s_branch .Lcont2
.Lback1:
  s_branch .Lcont1
.Lback0:
  s_branch .Lcont0
.Ls:
  s_waitcnt lgkmcnt(0)
  s_add_u32 s28, s6, s28
  s_addc_u32 s29, s7, 0
  s_add_u32 s30, s8, s30
  s_addc_u32 s31, s9, 0
  s_cmp_lt_u32 s2, 48
  s_cbranch_scc0 .Lh0s
  s_barrier
.Lh0s:
  s_branch .Lprodd
.Lprod:
  s_setprio 3
  s_and_b32 s38, s2, 3
  s_lshr_b32 s39, s2, 2
  v_lshlrev_b32 v177, 4, v204
  v_mul_u32_u24 v173, 96, v206
  v_lshl_add_u32 v172, v205, 4, v173
  v_lshl_add_u32 v173, v205, 3, v173
  v_add_u32_e64 v173, 64, v173
  v_mul_u32_u24 v175, 384, v206
  v_lshl_add_u32 v175, v205, 3, v175
  s_lshl_b32 s33, s39, 5
  v_add_u32_e64 v174, s33, v175
  s_mul_i32 s33, s38, 18432
  s_mul_i32 s34, s39, 1536
  s_waitcnt lgkmcnt(0)
  s_add_u32 s28, s6, s28
  s_addc_u32 s29, s7, 0
  s_add_u32 s30, s8, s30
  s_addc_u32 s31, s9, 0
  s_add_u32 s34, s34, s33
  s_add_u32 s14, s14, s34
  s_addc_u32 s15, s15, 0
  s_add_u32 s12, s12, s33
  s_addc_u32 s13, s13, 0
  s_add_u32 s40, s4, 3072
  s_addc_u32 s41, s5, 0
  v_mov_b32 v203, 61440
  global_load_dword v203, v203, s[24:25] sc1
  global_load_dwordx4 v[120:123], v172, s[14:15]
  global_load_dwordx2 v[124:125], v173, s[14:15]
  s_mov_b32 exec_hi, 0xffff
  s_mov_b32 m0, 119808
  s_nop 0
  global_load_lds_dwordx4 v177, s[40:41] offset:-3072
  s_mov_b32 m0, 119824
  s_nop 0
  global_load_lds_dwordx4 v177, s[40:41] offset:-2304
  s_mov_b32 m0, 119840
  s_nop 0
  global_load_lds_dwordx4 v177, s[40:41] offset:-1536
  s_mov_b32 m0, 119856
  s_nop 0
  global_load_lds_dwordx4 v177, s[40:41] offset:-768
  s_mov_b32 m0, 119872
  s_nop 0
  global_load_lds_dwordx4 v177, s[40:41] offset:0
  s_mov_b32 m0, 119888
  s_nop 0
  global_load_lds_dwordx4 v177, s[40:41] offset:768
  s_mov_b32 m0, 119904
  s_nop 0
  global_load_lds_dwordx4 v177, s[40:41] offset:1536
  s_mov_b32 m0, 119920
  s_nop 0
  global_load_lds_dwordx4 v177, s[40:41] offset:2304
  s_mov_b32 exec_hi, -1
  s_mov_b32 m0, 98304
  s_nop 0
  global_load_lds_dwordx4 v177, s[12:13] offset:0
  global_load_lds_dwordx4 v177, s[12:13] offset:1024
  global_load_lds_dwordx4 v177, s[12:13] offset:2048
  global_load_dwordx2 v[72:73], v174, s[20:21]
  s_add_u32 s20, s20, 0x1800
  s_addc_u32 s21, s21, 0x0
  global_load_dwordx2 v[74:75], v174, s[20:21]
  s_add_u32 s20, s20, 0x1800
  s_addc_u32 s21, s21, 0x0
  s_add_u32 s12, s12, 0xc00
  s_addc_u32 s13, s13, 0x0
  s_mov_b32 m0, 101376
  s_nop 0
  global_load_lds_dwordx4 v177, s[12:13] offset:0
  global_load_lds_dwordx4 v177, s[12:13] offset:1024
  global_load_lds_dwordx4 v177, s[12:13] offset:2048
  global_load_dwordx2 v[76:77], v174, s[20:21]
  s_add_u32 s20, s20, 0x1800
  s_addc_u32 s21, s21, 0x0
  global_load_dwordx2 v[78:79], v174, s[20:21]
  s_add_u32 s20, s20, 0x1800
  s_addc_u32 s21, s21, 0x0
  s_add_u32 s12, s12, 0xc00
  s_addc_u32 s13, s13, 0x0
  s_mov_b32 m0, 104448
  s_nop 0
  global_load_lds_dwordx4 v177, s[12:13] offset:0
  global_load_lds_dwordx4 v177, s[12:13] offset:1024
  global_load_lds_dwordx4 v177, s[12:13] offset:2048
  global_load_dwordx2 v[80:81], v174, s[20:21]
  s_add_u32 s20, s20, 0x1800
  s_addc_u32 s21, s21, 0x0
  global_load_dwordx2 v[82:83], v174, s[20:21]
  s_add_u32 s20, s20, 0x1800
  s_addc_u32 s21, s21, 0x0
  s_add_u32 s12, s12, 0xc00
  s_addc_u32 s13, s13, 0x0
  s_mov_b32 m0, 107520
  s_nop 0
  global_load_lds_dwordx4 v177, s[12:13] offset:0
  global_load_lds_dwordx4 v177, s[12:13] offset:1024
  global_load_lds_dwordx4 v177, s[12:13] offset:2048
  global_load_dwordx2 v[84:85], v174, s[20:21]
  s_add_u32 s20, s20, 0x1800
  s_addc_u32 s21, s21, 0x0
  global_load_dwordx2 v[86:87], v174, s[20:21]
  s_add_u32 s20, s20, 0x1800
  s_addc_u32 s21, s21, 0x0
  s_add_u32 s12, s12, 0xc00
  s_addc_u32 s13, s13, 0x0
  s_mov_b32 m0, 110592
  s_nop 0
  global_load_lds_dwordx4 v177, s[12:13] offset:0
  global_load_lds_dwordx4 v177, s[12:13] offset:1024
  global_load_lds_dwordx4 v177, s[12:13] offset:2048
  global_load_dwordx2 v[88:89], v174, s[20:21]
  s_add_u32 s20, s20, 0x1800
  s_addc_u32 s21, s21, 0x0
  global_load_dwordx2 v[90:91], v174, s[20:21]
  s_add_u32 s20, s20, 0x1800
  s_addc_u32 s21, s21, 0x0
  s_add_u32 s12, s12, 0xc00
  s_addc_u32 s13, s13, 0x0
  s_mov_b32 m0, 113664
  s_nop 0
  global_load_lds_dwordx4 v177, s[12:13] offset:0
  global_load_lds_dwordx4 v177, s[12:13] offset:1024
  global_load_lds_dwordx4 v177, s[12:13] offset:2048
  global_load_dwordx2 v[92:93], v174, s[20:21]
  s_add_u32 s20, s20, 0x1800
  s_addc_u32 s21, s21, 0x0
  global_load_dwordx2 v[94:95], v174, s[20:21]
  s_barrier
.Lprod2:
  v_lshrrev_b32_e64 v178, 1, v206
  v_lshl_add_u32 v178, v178, 4, v175
  v_add_u32 v178, 116736, v178
  v_add_u32 v179, 98304, v172
  v_add_u32 v180, 98304, v173
  v_lshlrev_b32 v207, 2, v205
  v_sub_u32 v207, v206, v207
  v_and_b32_e64 v208, 1, v206
  v_lshlrev_b32 v208, 4, v208
  s_movk_i32 s33, 0x3f00
  v_lshlrev_b32 v208, v208, s33
  v_ashrrev_i32 v207, 1, v207
  v_cmp_eq_i32 vcc, 0, v207
  s_nop 1
  v_cndmask_b32 v126, 0, v208, vcc
  v_cmp_eq_i32 vcc, 1, v207
  s_nop 1
  v_cndmask_b32_e64 v127, 0, v208, vcc
  v_bfe_u32 v207, v206, 2, 1
  v_mul_u32_u24 v207, 12, v207
  v_add_u32 v207, v207, v205
  v_lshlrev_b32 v207, 6, v207
  v_lshrrev_b32 v208, 3, v206
  v_lshl_add_u32 v207, v208, 2, v207
  v_and_b32 v208, 3, v206
  v_add_u32 v207, v207, v208
  s_lshr_b32 s33, s38, 1
  s_mul_i32 s33, s33, 24
  s_and_b32 s34, s39, 12
  s_add_u32 s33, s33, s34
  s_lshl_b32 s33, s33, 6
  s_and_b32 s34, s39, 3
  s_lshl_b32 s34, s34, 4
  s_add_u32 s33, s33, s34
  s_and_b32 s34, s38, 1
  s_lshl_b32 s34, s34, 3
  s_add_u32 s33, s33, s34
  v_add_lshl_u32 v176, v207, s33, 4
  s_waitcnt vmcnt(30)
  ds_read_b64 v[96:97], v178 offset:0
  ds_read_b64 v[98:99], v178 offset:32
  ds_read_b64 v[100:101], v178 offset:64
  ds_read_b64 v[102:103], v178 offset:96
  ds_read_b64 v[104:105], v178 offset:128
  ds_read_b64 v[106:107], v178 offset:160
  ds_read_b64 v[108:109], v178 offset:192
  ds_read_b64 v[110:111], v178 offset:224
  ds_read_b64 v[112:113], v178 offset:256
  ds_read_b64 v[114:115], v178 offset:288
  ds_read_b64 v[116:117], v178 offset:320
  ds_read_b64 v[118:119], v178 offset:352
  s_waitcnt vmcnt(27)
  ds_read_b128 v[0:3], v179 offset:0
  ds_read_b64 v[48:49], v180 offset:0
  ds_read_b128 v[4:7], v179 offset:1536
  ds_read_b64 v[50:51], v180 offset:1536
  s_waitcnt vmcnt(22)
  ds_read_b128 v[8:11], v179 offset:3072
  ds_read_b64 v[52:53], v180 offset:3072
  ds_read_b128 v[12:15], v179 offset:4608
  ds_read_b64 v[54:55], v180 offset:4608
  s_waitcnt lgkmcnt(7)
  v_mfma_f32_16x16x32_bf16 v[128:131], v[0:3], v[120:123], 0
  s_waitcnt lgkmcnt(5)
  v_mfma_f32_16x16x32_bf16 v[132:135], v[4:7], v[120:123], 0
  s_nop 0
  s_nop 2
  v_mfma_f32_16x16x16_bf16 v[128:131], v[48:49], v[124:125], v[128:131]
  s_waitcnt lgkmcnt(4)
  v_mfma_f32_16x16x16_bf16 v[132:135], v[50:51], v[124:125], v[132:135]
  v_mfma_f32_16x16x16_bf16 v[128:131], v[72:73], v[126:127], v[128:131]
  v_mfma_f32_16x16x16_bf16 v[132:135], v[74:75], v[126:127], v[132:135]
  s_waitcnt vmcnt(17)
  ds_read_b128 v[16:19], v179 offset:6144
  ds_read_b64 v[56:57], v180 offset:6144
  ds_read_b128 v[20:23], v179 offset:7680
  ds_read_b64 v[58:59], v180 offset:7680
  s_waitcnt lgkmcnt(7)
  v_mfma_f32_16x16x32_bf16 v[136:139], v[8:11], v[120:123], 0
  s_waitcnt lgkmcnt(5)
  v_mfma_f32_16x16x32_bf16 v[140:143], v[12:15], v[120:123], 0
  s_nop 3
  v_mfma_f32_16x16x16_bf16 v[136:139], v[52:53], v[124:125], v[136:139]
  s_waitcnt lgkmcnt(4)
  v_mfma_f32_16x16x16_bf16 v[140:143], v[54:55], v[124:125], v[140:143]
  v_mfma_f32_16x16x16_bf16 v[136:139], v[76:77], v[126:127], v[136:139]
  v_mfma_f32_16x16x16_bf16 v[140:143], v[78:79], v[126:127], v[140:143]
  v_cvt_pk_bf16_f32 v144, v128, v129
  v_cvt_pk_bf16_f32 v145, v130, v131
  v_cvt_pk_bf16_f32 v146, v132, v133
  v_cvt_pk_bf16_f32 v147, v134, v135
  v_mfma_f32_16x16x16_bf16 v[168:171], v[144:145], v[96:97], 0
  s_nop 0
  v_mfma_f32_16x16x16_bf16 v[168:171], v[146:147], v[98:99], v[168:171]
  s_waitcnt vmcnt(12)
  ds_read_b128 v[24:27], v179 offset:9216
  ds_read_b64 v[60:61], v180 offset:9216
  ds_read_b128 v[28:31], v179 offset:10752
  ds_read_b64 v[62:63], v180 offset:10752
  s_waitcnt lgkmcnt(7)
  v_mfma_f32_16x16x32_bf16 v[128:131], v[16:19], v[120:123], 0
  s_waitcnt lgkmcnt(5)
  v_mfma_f32_16x16x32_bf16 v[132:135], v[20:23], v[120:123], 0
  s_nop 3
  v_mfma_f32_16x16x16_bf16 v[128:131], v[56:57], v[124:125], v[128:131]
  s_waitcnt lgkmcnt(4)
  v_mfma_f32_16x16x16_bf16 v[132:135], v[58:59], v[124:125], v[132:135]
  v_mfma_f32_16x16x16_bf16 v[128:131], v[80:81], v[126:127], v[128:131]
  v_mfma_f32_16x16x16_bf16 v[132:135], v[82:83], v[126:127], v[132:135]
  v_cvt_pk_bf16_f32 v148, v136, v137
  v_cvt_pk_bf16_f32 v149, v138, v139
  v_cvt_pk_bf16_f32 v150, v140, v141
  v_cvt_pk_bf16_f32 v151, v142, v143
  v_mfma_f32_16x16x16_bf16 v[168:171], v[148:149], v[100:101], v[168:171]
  s_nop 0
  v_mfma_f32_16x16x16_bf16 v[168:171], v[150:151], v[102:103], v[168:171]
  s_waitcnt vmcnt(7)
  ds_read_b128 v[32:35], v179 offset:12288
  ds_read_b64 v[64:65], v180 offset:12288
  ds_read_b128 v[36:39], v179 offset:13824
  ds_read_b64 v[66:67], v180 offset:13824
  s_waitcnt lgkmcnt(7)
  v_mfma_f32_16x16x32_bf16 v[136:139], v[24:27], v[120:123], 0
  s_waitcnt lgkmcnt(5)
  v_mfma_f32_16x16x32_bf16 v[140:143], v[28:31], v[120:123], 0
  s_nop 3
  v_mfma_f32_16x16x16_bf16 v[136:139], v[60:61], v[124:125], v[136:139]
  s_waitcnt lgkmcnt(4)
  v_mfma_f32_16x16x16_bf16 v[140:143], v[62:63], v[124:125], v[140:143]
  v_mfma_f32_16x16x16_bf16 v[136:139], v[84:85], v[126:127], v[136:139]
  v_mfma_f32_16x16x16_bf16 v[140:143], v[86:87], v[126:127], v[140:143]
  v_cvt_pk_bf16_f32 v152, v128, v129
  v_cvt_pk_bf16_f32 v153, v130, v131
  v_cvt_pk_bf16_f32 v154, v132, v133
  v_cvt_pk_bf16_f32 v155, v134, v135
  v_mfma_f32_16x16x16_bf16 v[168:171], v[152:153], v[104:105], v[168:171]
  s_nop 0
  v_mfma_f32_16x16x16_bf16 v[168:171], v[154:155], v[106:107], v[168:171]
  s_waitcnt vmcnt(2)
  ds_read_b128 v[40:43], v179 offset:15360
  ds_read_b64 v[68:69], v180 offset:15360
  ds_read_b128 v[44:47], v179 offset:16896
  ds_read_b64 v[70:71], v180 offset:16896
  s_waitcnt lgkmcnt(7)
  v_mfma_f32_16x16x32_bf16 v[128:131], v[32:35], v[120:123], 0
  s_waitcnt lgkmcnt(5)
  v_mfma_f32_16x16x32_bf16 v[132:135], v[36:39], v[120:123], 0
  s_nop 3
  v_mfma_f32_16x16x16_bf16 v[128:131], v[64:65], v[124:125], v[128:131]
  s_waitcnt lgkmcnt(4)
  v_mfma_f32_16x16x16_bf16 v[132:135], v[66:67], v[124:125], v[132:135]
  v_mfma_f32_16x16x16_bf16 v[128:131], v[88:89], v[126:127], v[128:131]
  v_mfma_f32_16x16x16_bf16 v[132:135], v[90:91], v[126:127], v[132:135]
  v_cvt_pk_bf16_f32 v156, v136, v137
  v_cvt_pk_bf16_f32 v157, v138, v139
  v_cvt_pk_bf16_f32 v158, v140, v141
  v_cvt_pk_bf16_f32 v159, v142, v143
  v_mfma_f32_16x16x16_bf16 v[168:171], v[156:157], v[108:109], v[168:171]
  s_nop 0
  v_mfma_f32_16x16x16_bf16 v[168:171], v[158:159], v[110:111], v[168:171]
  s_waitcnt lgkmcnt(3)
  v_mfma_f32_16x16x32_bf16 v[136:139], v[40:43], v[120:123], 0
  s_waitcnt lgkmcnt(1)
  v_mfma_f32_16x16x32_bf16 v[140:143], v[44:47], v[120:123], 0
  s_nop 3
  v_mfma_f32_16x16x16_bf16 v[136:139], v[68:69], v[124:125], v[136:139]
  s_waitcnt lgkmcnt(0)
  v_mfma_f32_16x16x16_bf16 v[140:143], v[70:71], v[124:125], v[140:143]
  s_waitcnt vmcnt(1)
  v_mfma_f32_16x16x16_bf16 v[136:139], v[92:93], v[126:127], v[136:139]
  s_waitcnt vmcnt(0)
  v_mfma_f32_16x16x16_bf16 v[140:143], v[94:95], v[126:127], v[140:143]
  v_cvt_pk_bf16_f32 v160, v128, v129
  v_cvt_pk_bf16_f32 v161, v130, v131
  v_cvt_pk_bf16_f32 v162, v132, v133
  v_cvt_pk_bf16_f32 v163, v134, v135
  v_mfma_f32_16x16x16_bf16 v[168:171], v[160:161], v[112:113], v[168:171]
  s_nop 0
  v_mfma_f32_16x16x16_bf16 v[168:171], v[162:163], v[114:115], v[168:171]
  v_cvt_pk_bf16_f32 v164, v136, v137
  v_cvt_pk_bf16_f32 v165, v138, v139
  v_cvt_pk_bf16_f32 v166, v140, v141
  v_cvt_pk_bf16_f32 v167, v142, v143
  v_mfma_f32_16x16x16_bf16 v[168:171], v[164:165], v[116:117], v[168:171]
  s_nop 0
  v_mfma_f32_16x16x16_bf16 v[168:171], v[166:167], v[118:119], v[168:171]
  v_readfirstlane_b32 s54, v203
  s_add_u32 s54, s54, 1
  s_nop 0
  v_mov_b32 v1, s54
  v_mov_b32 v3, s54
  s_nop 0
  s_nop 1
  v_mul_f32 v168, 0x3e553b95, v168
  v_mul_f32 v169, 0x3e553b95, v169
  v_mul_f32 v170, 0x3e553b95, v170
  v_mul_f32 v171, 0x3e553b95, v171
  v_cvt_pk_bf16_f32 v0, v168, v169
  v_cvt_pk_bf16_f32 v2, v170, v171
  global_store_dwordx4 v176, v[0:3], s[24:25] sc1
  s_setprio 0
.Lprodd:
  v_mov_b32 v224, 0
  v_mov_b32 v225, 0
  v_mov_b32 v226, 0
  v_mov_b32_e64 v227, 0
  v_mov_b32 v212, 159744
  ds_write_b128 v212, v[224:227] offset:0
  ds_write_b128 v212, v[224:227] offset:16
  s_mov_b32 s33, 0x75643120
  v_and_b32 v207, 14, v206
  v_lshlrev_b32 v207, 1, v207
  v_bfe_u32 v207, s33, v207, 3
  v_lshlrev_b32 v208, 1, v205
  v_xor_b32 v207, v207, v208
  v_lshlrev_b32_e64 v207, 4, v207
  v_mul_u32_u24 v208, 384, v206
  v_add3_u32 v240, v207, v208, s42
  v_xor_b32 v241, 16, v240
  v_lshrrev_b32 v207, 1, v206
  v_and_b32 v207, 4, v207
  v_and_b32 v208, 7, v206
  v_xor_b32 v207, v207, v208
  v_xor_b32 v207, v207, v205
  v_lshlrev_b32 v207, 4, v207
  v_lshl_add_u32 v207, v206, 7, v207
  s_lshl_b32 s34, s27, 11
  s_add_u32 s34, s34, 98304
  v_add_u32 v242, s34, v207
  v_xad_u32 v243, v207, 64, s34
  s_lshl_b32 s34, s27, 5
  s_add_u32 s34, s34, 157696
  v_lshl_add_u32 v244, v206, 1, s34
  v_and_b32 v207, 1, v205
  v_lshlrev_b32 v207, 7, v207
  v_lshl_add_u32 v207, v205, 9, v207
  v_lshrrev_b32_e64 v208, 2, v206
  v_lshl_add_u32 v207, v208, 5, v207
  v_and_b32_e64 v208, 3, v206
  v_lshl_add_u32 v207, v208, 3, v207
  s_mul_i32 s34, s27, 6144
  s_add_u32 s34, s34, 133120
  v_add_u32_e64 v245, s34, v207
  v_xor_b32 v207, 144, v207
  v_add_u32_e64 v246, s34, v207
  s_mul_i32 s34, s27, 12288
  v_lshl_add_u32 v104, v204, 4, s34
  v_add_u32 v105, 4096, v104
  v_add_u32 v106, 8192, v104
  v_lshlrev_b32_e64 v108, 4, v204
  v_add_u32 v108, 133120, v108
  s_mul_i32 s34, s27, 6144
  v_add_u32 v107, s34, v108
  s_movk_i32 s33, 0x3f80
  v_and_b32 v207, 1, v206
  v_lshlrev_b32 v208, 4, v207
  v_lshlrev_b32 v208, v208, s33
  v_and_b32 v209, 2, v206
  v_cmp_eq_u32 vcc, 0, v209
  s_nop 1
  v_cndmask_b32 v238, 0, v208, vcc
  v_xor_b32_e64 v239, v238, v208
  v_mul_u32_u24 v207, 0xaaaa, v207
  v_add_u32 v207, 0x4444, v207
  v_lshl_or_b32 v236, v207, 16, v207
  v_mov_b32 v232, 0x3f803f80
  v_mov_b32 v233, 0x3f803f80
  v_mov_b32 v234, 0x3f803f80
  v_mov_b32 v235, 0x3f803f80
  v_mov_b32 v218, 0
  v_mov_b32 v219, 0
  s_mov_b32 s48, 0xaaaaaaaa
  s_mov_b32 s49, 0xaaaaaaaa
  s_mov_b32 s50, 0xcccccccc
  s_mov_b32 s51, 0xcccccccc
  s_mov_b32 s52, 0xf0f0f0f0
  s_mov_b32 s53, 0xf0f0f0f0
  s_mov_b32 s56, 0xff00ff00
  s_mov_b32 s57, 0xff00ff00
  s_waitcnt lgkmcnt(0)
  s_barrier
  s_barrier
  v_mov_b32 v203, 61440
  global_load_dword v203, v203, s[24:25] sc1
  s_barrier
  s_waitcnt vmcnt(0)
  v_readfirstlane_b32 s54, v203
  s_add_u32 s54, s54, 1
  s_mov_b32 s93, 0
  v_mov_b32 v248, 159744
  v_mov_b32 v249, 0x3baaaaab
  v_lshrrev_b32_e64 v250, 2, v206
  v_lshl_add_u32 v250, v250, 4, v206
  v_lshlrev_b32 v250, 2, v250
  s_movk_i32 s55, -8192
  s_waitcnt lgkmcnt(0)
  s_barrier
  global_load_dwordx4 v[0:3], v104, s[24:25] offset:0 sc1
  global_load_dwordx4 v[4:7], v104, s[24:25] offset:1024 sc1
  global_load_dwordx4 v[8:11], v104, s[24:25] offset:2048 sc1
  global_load_dwordx4 v[12:15], v104, s[24:25] offset:3072 sc1
  global_load_dwordx4 v[16:19], v105, s[24:25] offset:0 sc1
  global_load_dwordx4 v[20:23], v105, s[24:25] offset:1024 sc1
  global_load_dwordx4 v[24:27], v105, s[24:25] offset:2048 sc1
  global_load_dwordx4 v[28:31], v105, s[24:25] offset:3072 sc1
  global_load_dwordx4 v[32:35], v106, s[24:25] offset:0 sc1
  global_load_dwordx4 v[36:39], v106, s[24:25] offset:1024 sc1
  global_load_dwordx4 v[40:43], v106, s[24:25] offset:2048 sc1
  global_load_dwordx4 v[44:47], v106, s[24:25] offset:3072 sc1
  ds_read_b64_tr_b16 v[152:153], v245 offset:0
  ds_read_b64_tr_b16 v[154:155], v246 offset:0
  ds_read_b128 v[176:179], v240 offset:0
  ds_read_b64_tr_b16 v[156:157], v245 offset:256
  ds_read_b64_tr_b16 v[158:159], v246 offset:256
  ds_read_b128 v[180:183], v241 offset:0
  ds_read_b64_tr_b16 v[160:161], v245 offset:2048
  ds_read_b64_tr_b16 v[162:163], v246 offset:2048
  ds_read_b128 v[184:187], v240 offset:128
  ds_read_b64_tr_b16 v[164:165], v245 offset:2304
  ds_read_b64_tr_b16 v[166:167], v246 offset:2304
  ds_read_b128 v[188:191], v241 offset:128
  ds_read_b64_tr_b16 v[168:169], v245 offset:4096
  ds_read_b64_tr_b16 v[170:171], v246 offset:4096
  ds_read_b128 v[192:195], v240 offset:256
  ds_read_b64_tr_b16 v[172:173], v245 offset:4352
  ds_read_b64_tr_b16 v[124:125], v246 offset:4352
  ds_read_b128 v[196:199], v241 offset:256
  v_mov_b64 v[216:217], 0
  s_waitcnt lgkmcnt(15)
  v_mfma_f32_4x4x4_16b_bf16 v[128:131], v[238:239], v[152:153], 0
  v_mfma_f32_4x4x4_16b_bf16 v[132:135], v[238:239], v[154:155], 0
  s_nop 0
  v_mfma_f32_4x4x4_16b_bf16 v[200:203], v[238:239], v[176:177], v[128:131]
  v_mfma_f32_4x4x4_16b_bf16 v[204:207], v[238:239], v[178:179], v[132:135]
  s_waitcnt lgkmcnt(14)
  v_mfma_f32_4x4x4_16b_bf16 v[136:139], v[238:239], v[156:157], 0
  s_waitcnt lgkmcnt(13)
  v_mfma_f32_4x4x4_16b_bf16 v[140:143], v[238:239], v[158:159], 0
  s_waitcnt lgkmcnt(12)
  v_mfma_f32_4x4x4_16b_bf16 v[208:211], v[238:239], v[180:181], v[136:139]
  v_mfma_f32_4x4x4_16b_bf16 v[220:223], v[238:239], v[182:183], v[140:143]
  s_waitcnt lgkmcnt(11)
  v_mfma_f32_4x4x4_16b_bf16 v[144:147], v[238:239], v[160:161], 0
  s_waitcnt lgkmcnt(10)
  v_mfma_f32_4x4x4_16b_bf16 v[148:151], v[238:239], v[162:163], 0
  s_waitcnt lgkmcnt(9)
  v_mfma_f32_4x4x4_16b_bf16 v[224:227], v[238:239], v[184:185], v[144:147]
  v_mfma_f32_4x4x4_16b_bf16 v[228:231], v[238:239], v[186:187], v[148:151]
  v_cvt_pk_bf16_f32 v176, v200, v201
  v_cvt_pk_bf16_f32 v177, v202, v203
  v_cvt_pk_bf16_f32 v178, v204, v205
  v_cvt_pk_bf16_f32 v179, v206, v207
  s_waitcnt lgkmcnt(8)
  v_mfma_f32_4x4x4_16b_bf16 v[152:155], v[238:239], v[164:165], 0
  s_waitcnt lgkmcnt(7)
  v_mfma_f32_4x4x4_16b_bf16 v[156:159], v[238:239], v[166:167], 0
  s_waitcnt lgkmcnt(6)
  v_mfma_f32_4x4x4_16b_bf16 v[200:203], v[238:239], v[188:189], v[152:155]
  v_mfma_f32_4x4x4_16b_bf16 v[204:207], v[238:239], v[190:191], v[156:159]
  v_mfma_f32_16x16x32_bf16 v[212:215], v[176:179], v[176:179], 0
  ds_write_b128 v240, v[176:179] offset:0
  v_cvt_pk_bf16_f32 v180, v208, v209
  v_cvt_pk_bf16_f32 v181, v210, v211
  v_cvt_pk_bf16_f32 v182, v220, v221
  v_cvt_pk_bf16_f32 v183, v222, v223
  s_waitcnt lgkmcnt(6)
  v_mfma_f32_4x4x4_16b_bf16 v[160:163], v[238:239], v[168:169], 0
  s_waitcnt lgkmcnt(5)
  v_mfma_f32_4x4x4_16b_bf16 v[164:167], v[238:239], v[170:171], 0
  s_waitcnt lgkmcnt(4)
  v_mfma_f32_4x4x4_16b_bf16 v[208:211], v[238:239], v[192:193], v[160:163]
  v_mfma_f32_4x4x4_16b_bf16 v[220:223], v[238:239], v[194:195], v[164:167]
  v_mfma_f32_16x16x32_bf16 v[212:215], v[180:183], v[180:183], v[212:215]
  v_smfmac_f32_16x16x64_bf16 v[216:219], v[232:235], v[176:183], v236
  ds_write_b128 v241, v[180:183] offset:0
  v_cvt_pk_bf16_f32 v184, v224, v225
  v_cvt_pk_bf16_f32 v185, v226, v227
  v_cvt_pk_bf16_f32 v186, v228, v229
  v_cvt_pk_bf16_f32 v187, v230, v231
  s_waitcnt lgkmcnt(4)
  v_mfma_f32_4x4x4_16b_bf16 v[168:171], v[238:239], v[172:173], 0
  s_waitcnt lgkmcnt(3)
  v_mfma_f32_4x4x4_16b_bf16 v[172:175], v[238:239], v[124:125], 0
  s_waitcnt lgkmcnt(2)
  v_mfma_f32_4x4x4_16b_bf16 v[224:227], v[238:239], v[196:197], v[168:171]
  v_mfma_f32_4x4x4_16b_bf16 v[228:231], v[238:239], v[198:199], v[172:175]
  v_mfma_f32_16x16x32_bf16 v[212:215], v[184:187], v[184:187], v[212:215]
  ds_write_b128 v240, v[184:187] offset:128
  v_cvt_pk_bf16_f32 v188, v200, v201
  v_cvt_pk_bf16_f32 v189, v202, v203
  v_cvt_pk_bf16_f32 v190, v204, v205
  v_cvt_pk_bf16_f32 v191, v206, v207
  s_nop 0
  s_nop 0
  v_mfma_f32_16x16x32_bf16 v[212:215], v[188:191], v[188:191], v[212:215]
  v_smfmac_f32_16x16x64_bf16 v[216:219], v[232:235], v[184:191], v236
  ds_write_b128 v241, v[188:191] offset:128
  v_cvt_pk_bf16_f32 v192, v208, v209
  v_cvt_pk_bf16_f32 v193, v210, v211
  v_cvt_pk_bf16_f32 v194, v220, v221
  v_cvt_pk_bf16_f32 v195, v222, v223
  s_nop 0
  s_nop 0
  v_mfma_f32_16x16x32_bf16 v[212:215], v[192:195], v[192:195], v[212:215]
  ds_write_b128 v240, v[192:195] offset:256
  v_cvt_pk_bf16_f32 v196, v224, v225
  v_cvt_pk_bf16_f32 v197, v226, v227
  v_cvt_pk_bf16_f32 v198, v228, v229
  v_cvt_pk_bf16_f32 v199, v230, v231
  s_nop 1
  v_mfma_f32_16x16x32_bf16 v[212:215], v[196:199], v[196:199], v[212:215]
  v_smfmac_f32_16x16x64_bf16 v[216:219], v[232:235], v[192:199], v236
  ds_write_b128 v241, v[196:199] offset:256
  s_branch .Lgc
.Lga:
  global_load_dwordx4 v[0:3], v104, s[24:25] offset:0 sc1
  global_load_dwordx4 v[4:7], v104, s[24:25] offset:1024 sc1
  global_load_dwordx4 v[8:11], v104, s[24:25] offset:2048 sc1
  global_load_dwordx4 v[12:15], v104, s[24:25] offset:3072 sc1
  global_load_dwordx4 v[16:19], v105, s[24:25] offset:0 sc1
  global_load_dwordx4 v[20:23], v105, s[24:25] offset:1024 sc1
  global_load_dwordx4 v[24:27], v105, s[24:25] offset:2048 sc1
  global_load_dwordx4 v[28:31], v105, s[24:25] offset:3072 sc1
  global_load_dwordx4 v[32:35], v106, s[24:25] offset:0 sc1
  global_load_dwordx4 v[36:39], v106, s[24:25] offset:1024 sc1
  global_load_dwordx4 v[40:43], v106, s[24:25] offset:2048 sc1
  global_load_dwordx4 v[44:47], v106, s[24:25] offset:3072 sc1
.Lgc:
  s_waitcnt vmcnt(11)
  v_lshlrev_b32 v48, 16, v0
  v_and_b32 v49, 0xffff0000, v0
  v_lshlrev_b32_e64 v50, 16, v2
  v_and_b32 v51, 0xffff0000, v2
  v_bitop3_b32 v101, v1, v3, s54 bitop3:0x7e
  v_pk_add_f32 v[96:97], v[48:49], v[50:51]
  s_waitcnt vmcnt(10)
  v_lshlrev_b32 v52, 16, v4
  v_and_b32 v53, 0xffff0000, v4
  v_lshlrev_b32_e64 v54, 16, v6
  v_and_b32 v55, 0xffff0000, v6
  v_bitop3_b32 v102, v5, v7, s54 bitop3:0x7e
  v_pk_add_f32 v[98:99], v[52:53], v[54:55]
  v_or3_b32 v100, 0, v101, v102
  v_pk_add_f32 v[96:97], v[96:97], v[98:99]
  s_waitcnt vmcnt(9)
  v_lshlrev_b32 v56, 16, v8
  v_and_b32 v57, 0xffff0000, v8
  v_lshlrev_b32_e64 v58, 16, v10
  v_and_b32 v59, 0xffff0000, v10
  v_bitop3_b32 v101, v9, v11, s54 bitop3:0x7e
  v_pk_add_f32 v[98:99], v[56:57], v[58:59]
  v_pk_add_f32 v[96:97], v[96:97], v[98:99]
  s_waitcnt vmcnt(8)
  v_lshlrev_b32 v60, 16, v12
  v_and_b32 v61, 0xffff0000, v12
  v_lshlrev_b32_e64 v62, 16, v14
  v_and_b32 v63, 0xffff0000, v14
  v_bitop3_b32 v102, v13, v15, s54 bitop3:0x7e
  v_pk_add_f32 v[98:99], v[60:61], v[62:63]
  v_or3_b32 v100, v100, v101, v102
  v_pk_add_f32 v[96:97], v[96:97], v[98:99]
  s_waitcnt vmcnt(7)
  v_lshlrev_b32 v64, 16, v16
  v_and_b32 v65, 0xffff0000, v16
  v_lshlrev_b32_e64 v66, 16, v18
  v_and_b32 v67, 0xffff0000, v18
  v_bitop3_b32 v101, v17, v19, s54 bitop3:0x7e
  v_pk_add_f32 v[98:99], v[64:65], v[66:67]
  v_pk_add_f32 v[96:97], v[96:97], v[98:99]
  s_waitcnt vmcnt(6)
  v_lshlrev_b32 v68, 16, v20
  v_and_b32 v69, 0xffff0000, v20
  v_lshlrev_b32_e64 v70, 16, v22
  v_and_b32 v71, 0xffff0000, v22
  v_bitop3_b32 v102, v21, v23, s54 bitop3:0x7e
  v_pk_add_f32 v[98:99], v[68:69], v[70:71]
  v_or3_b32 v100, v100, v101, v102
  v_pk_add_f32 v[96:97], v[96:97], v[98:99]
  s_waitcnt vmcnt(5)
  v_lshlrev_b32 v72, 16, v24
  v_and_b32 v73, 0xffff0000, v24
  v_lshlrev_b32_e64 v74, 16, v26
  v_and_b32 v75, 0xffff0000, v26
  v_bitop3_b32 v101, v25, v27, s54 bitop3:0x7e
  v_pk_add_f32 v[98:99], v[72:73], v[74:75]
  v_pk_add_f32 v[96:97], v[96:97], v[98:99]
  s_waitcnt vmcnt(4)
  v_lshlrev_b32 v76, 16, v28
  v_and_b32 v77, 0xffff0000, v28
  v_lshlrev_b32_e64 v78, 16, v30
  v_and_b32 v79, 0xffff0000, v30
  v_bitop3_b32 v102, v29, v31, s54 bitop3:0x7e
  v_pk_add_f32 v[98:99], v[76:77], v[78:79]
  v_or3_b32 v100, v100, v101, v102
  v_pk_add_f32 v[96:97], v[96:97], v[98:99]
  s_waitcnt vmcnt(3)
  v_lshlrev_b32 v80, 16, v32
  v_and_b32 v81, 0xffff0000, v32
  v_lshlrev_b32_e64 v82, 16, v34
  v_and_b32 v83, 0xffff0000, v34
  v_bitop3_b32 v101, v33, v35, s54 bitop3:0x7e
  v_pk_add_f32 v[98:99], v[80:81], v[82:83]
  v_pk_add_f32 v[96:97], v[96:97], v[98:99]
  s_waitcnt vmcnt(2)
  v_lshlrev_b32 v84, 16, v36
  v_and_b32 v85, 0xffff0000, v36
  v_lshlrev_b32_e64 v86, 16, v38
  v_and_b32 v87, 0xffff0000, v38
  v_bitop3_b32 v102, v37, v39, s54 bitop3:0x7e
  v_pk_add_f32 v[98:99], v[84:85], v[86:87]
  v_or3_b32 v100, v100, v101, v102
  v_pk_add_f32 v[96:97], v[96:97], v[98:99]
  s_waitcnt vmcnt(1)
  v_lshlrev_b32 v88, 16, v40
  v_and_b32 v89, 0xffff0000, v40
  v_lshlrev_b32_e64 v90, 16, v42
  v_and_b32 v91, 0xffff0000, v42
  v_bitop3_b32 v101, v41, v43, s54 bitop3:0x7e
  v_pk_add_f32 v[98:99], v[88:89], v[90:91]
  v_pk_add_f32 v[96:97], v[96:97], v[98:99]
  s_waitcnt vmcnt(0)
  v_lshlrev_b32 v92, 16, v44
  v_and_b32 v93, 0xffff0000, v44
  v_lshlrev_b32_e64 v94, 16, v46
  v_and_b32 v95, 0xffff0000, v46
  v_bitop3_b32 v102, v45, v47, s54 bitop3:0x7e
  v_pk_add_f32 v[98:99], v[92:93], v[94:95]
  v_or3_b32 v100, v100, v101, v102
  v_pk_add_f32 v[96:97], v[96:97], v[98:99]
  v_cmp_ne_u32 vcc, 0, v100
  s_add_u32 s93, s93, 1
  s_cbranch_vccz .Lgad
  s_mov_b64 exec, vcc
  s_cmp_lt_u32 s93, 32768
  s_cbranch_scc1 .Lga
.Lgad:
  s_mov_b64 exec, -1
  v_mov_b32 v252, 1
  s_mov_b64 exec, 1
  ds_add_u32 v248, v252 offset:16
  s_mov_b64 exec, -1
  v_add_f32 v96, v96, v97
  v_mov_b32 v97, v96
  s_nop 1
  v_permlane32_swap_b32 v96, v97
  v_add_f32 v96, v96, v97
  v_mov_b32 v97, v96
  s_nop 1
  v_permlane16_swap_b32 v96, v97
  v_add_f32 v96, v96, v97
  v_mul_f32 v96, 0xbbaaaaab, v96
  v_pk_add_f32 v[48:49], v[48:49], v[96:97] op_sel_hi:[1,0]
  v_pk_add_f32 v[50:51], v[50:51], v[96:97] op_sel_hi:[1,0]
  v_pk_add_f32 v[52:53], v[52:53], v[96:97] op_sel_hi:[1,0]
  v_pk_add_f32 v[54:55], v[54:55], v[96:97] op_sel_hi:[1,0]
  v_cvt_pk_bf16_f32 v0, v48, v49
  v_cvt_pk_bf16_f32 v1, v50, v51
  v_cvt_pk_bf16_f32 v2, v52, v53
  v_cvt_pk_bf16_f32 v3, v54, v55
  ds_write_b128 v107, v[0:3] offset:0
  v_pk_add_f32 v[56:57], v[56:57], v[96:97] op_sel_hi:[1,0]
  v_pk_add_f32 v[58:59], v[58:59], v[96:97] op_sel_hi:[1,0]
  v_pk_add_f32 v[60:61], v[60:61], v[96:97] op_sel_hi:[1,0]
  v_pk_add_f32 v[62:63], v[62:63], v[96:97] op_sel_hi:[1,0]
  v_cvt_pk_bf16_f32 v4, v56, v57
  v_cvt_pk_bf16_f32 v5, v58, v59
  v_cvt_pk_bf16_f32 v6, v60, v61
  v_cvt_pk_bf16_f32 v7, v62, v63
  ds_write_b128 v107, v[4:7] offset:1024
  v_pk_add_f32 v[64:65], v[64:65], v[96:97] op_sel_hi:[1,0]
  v_pk_add_f32 v[66:67], v[66:67], v[96:97] op_sel_hi:[1,0]
  v_pk_add_f32 v[68:69], v[68:69], v[96:97] op_sel_hi:[1,0]
  v_pk_add_f32 v[70:71], v[70:71], v[96:97] op_sel_hi:[1,0]
  v_cvt_pk_bf16_f32 v8, v64, v65
  v_cvt_pk_bf16_f32 v9, v66, v67
  v_cvt_pk_bf16_f32 v10, v68, v69
  v_cvt_pk_bf16_f32 v11, v70, v71
  ds_write_b128 v107, v[8:11] offset:2048
  v_pk_add_f32 v[72:73], v[72:73], v[96:97] op_sel_hi:[1,0]
  v_pk_add_f32 v[74:75], v[74:75], v[96:97] op_sel_hi:[1,0]
  v_pk_add_f32 v[76:77], v[76:77], v[96:97] op_sel_hi:[1,0]
  v_pk_add_f32 v[78:79], v[78:79], v[96:97] op_sel_hi:[1,0]
  v_cvt_pk_bf16_f32 v12, v72, v73
  v_cvt_pk_bf16_f32 v13, v74, v75
  v_cvt_pk_bf16_f32 v14, v76, v77
  v_cvt_pk_bf16_f32 v15, v78, v79
  ds_write_b128 v107, v[12:15] offset:3072
  v_pk_add_f32 v[80:81], v[80:81], v[96:97] op_sel_hi:[1,0]
  v_pk_add_f32 v[82:83], v[82:83], v[96:97] op_sel_hi:[1,0]
  v_pk_add_f32 v[84:85], v[84:85], v[96:97] op_sel_hi:[1,0]
  v_pk_add_f32 v[86:87], v[86:87], v[96:97] op_sel_hi:[1,0]
  v_cvt_pk_bf16_f32 v16, v80, v81
  v_cvt_pk_bf16_f32 v17, v82, v83
  v_cvt_pk_bf16_f32 v18, v84, v85
  v_cvt_pk_bf16_f32 v19, v86, v87
  ds_write_b128 v107, v[16:19] offset:4096
  v_pk_add_f32 v[88:89], v[88:89], v[96:97] op_sel_hi:[1,0]
  v_pk_add_f32 v[90:91], v[90:91], v[96:97] op_sel_hi:[1,0]
  v_pk_add_f32 v[92:93], v[92:93], v[96:97] op_sel_hi:[1,0]
  v_pk_add_f32 v[94:95], v[94:95], v[96:97] op_sel_hi:[1,0]
  v_cvt_pk_bf16_f32 v20, v88, v89
  v_cvt_pk_bf16_f32 v21, v90, v91
  v_cvt_pk_bf16_f32 v22, v92, v93
  v_cvt_pk_bf16_f32 v23, v94, v95
  ds_write_b128 v107, v[20:23] offset:5120
  s_mov_b64 exec, 1
  ds_add_u32 v248, v252 offset:8
  s_mov_b64 exec, -1
  s_movk_i32 s93, 6144
  v_add_u32 v240, s93, v240
  v_add_u32_e64 v241, s93, v241
  v_cndmask_b32 v252, v212, v213, s[48:49]
  v_cndmask_b32 v253, v214, v215, s[48:49]
  v_add_f32_e64 v254, v216, v217
  v_cndmask_b32 v252, v252, v253, s[50:51]
  v_mul_f32 v254, v254, v254
  v_mov_b32 v253, v252
  s_nop 1
  v_permlane32_swap_b32 v252, v253
  v_cndmask_b32 v252, v252, v253, s[56:57]
  v_mov_b32_e64 v253, v252
  s_nop 1
  v_permlane16_swap_b32 v252, v253
  v_cndmask_b32 v252, v252, v253, s[52:53]
  v_fma_f32 v252, -v254, v249, v252
  v_max_f32_e64 v252, 0, v252
  v_fmaak_f32 v252, v249, v252, 0x3727c5ac
  v_rsq_f32 v247, v252
  s_nop 0
  v_mul_f32_e64 v253, v252, v247
  v_and_b32 v254, 0xffff0000, v253
  ds_write_b16_d16_hi v244, v253 offset:0
  v_sub_f32 v254, v253, v254
  v_cvt_pk_bf16_f32 v254, v254, v254
  ds_write_b16 v244, v254 offset:128
  s_mov_b32 s33, 0
.Lsq:
  ds_read_b32 v253, v248 offset:8
  s_add_u32 s33, s33, 1
  s_waitcnt lgkmcnt(0)
  v_readfirstlane_b32 s34, v253
  s_cmp_ge_u32 s33, 0x100000
  s_cbranch_scc1 .Lsqd
  s_cmp_lt_u32 s34, 4
  s_cbranch_scc1 .Lsq
.Lsqd:
  ds_read_b128 v[0:3], v108 offset:0
  ds_read_b128 v[4:7], v108 offset:1024
  ds_read_b128 v[8:11], v108 offset:2048
  ds_read_b128 v[12:15], v108 offset:3072
  ds_read_b128 v[16:19], v108 offset:4096
  ds_read_b128 v[20:23], v108 offset:5120
  ds_read_b128 v[24:27], v108 offset:6144
  ds_read_b128 v[28:31], v108 offset:7168
  ds_read_b128 v[32:35], v108 offset:8192
  ds_read_b128 v[36:39], v108 offset:9216
  ds_read_b128 v[40:43], v108 offset:10240
  ds_read_b128 v[44:47], v108 offset:11264
  ds_read_b128 v[48:51], v108 offset:12288
  ds_read_b128 v[52:55], v108 offset:13312
  ds_read_b128 v[56:59], v108 offset:14336
  ds_read_b128 v[60:63], v108 offset:15360
  ds_read_b128 v[64:67], v108 offset:16384
  ds_read_b128 v[68:71], v108 offset:17408
  ds_read_b128 v[72:75], v108 offset:18432
  ds_read_b128 v[76:79], v108 offset:19456
  ds_read_b128 v[80:83], v108 offset:20480
  ds_read_b128 v[84:87], v108 offset:21504
  ds_read_b128 v[88:91], v108 offset:22528
  ds_read_b128 v[92:95], v108 offset:23552
  s_waitcnt lgkmcnt(15)
  v_mfma_f32_16x16x32_bf16 v[112:115], v[0:3], v[176:179], 0
  v_mbcnt_lo_u32_b32 v251, -1, 0
  v_mbcnt_hi_u32_b32 v251, -1, v251
  v_lshrrev_b32 v255, 4, v251
  v_mfma_f32_16x16x32_bf16 v[112:115], v[4:7], v[180:183], v[112:115]
  v_lshlrev_b32 v251, 2, v251
  v_lshlrev_b32 v255, 5, v255
  v_add_u32 v251, 158208, v251
  v_mfma_f32_16x16x32_bf16 v[112:115], v[8:11], v[184:187], v[112:115]
  v_add_u32 v255, 158208, v255
  v_mfma_f32_16x16x32_bf16 v[112:115], v[12:15], v[188:191], v[112:115]
  v_mfma_f32_16x16x32_bf16 v[112:115], v[16:19], v[192:195], v[112:115]
  v_mfma_f32_16x16x32_bf16 v[112:115], v[20:23], v[196:199], v[112:115]
  v_mfma_f32_16x16x32_bf16 v[116:119], v[24:27], v[176:179], 0
  v_mfma_f32_16x16x32_bf16 v[116:119], v[28:31], v[180:183], v[116:119]
  s_nop 5
  v_mul_f32 v112, v112, v247
  v_mul_f32 v113, v113, v247
  v_mul_f32 v114, v114, v247
  v_mfma_f32_16x16x32_bf16 v[116:119], v[32:35], v[184:187], v[116:119]
  v_mul_f32 v115, v115, v247
  v_max_f32_dpp v96, v112, v112 row_ror:8 row_mask:0xf bank_mask:0xf
  v_max_f32_dpp v97, v113, v113 row_ror:8 row_mask:0xf bank_mask:0xf
  s_waitcnt lgkmcnt(14)
  v_mfma_f32_16x16x32_bf16 v[116:119], v[36:39], v[188:191], v[116:119]
  v_max_f32_dpp v98, v114, v114 row_ror:8 row_mask:0xf bank_mask:0xf
  v_max_f32_dpp v99, v115, v115 row_ror:8 row_mask:0xf bank_mask:0xf
  v_max_f32_dpp v96, v96, v96 row_ror:4 row_mask:0xf bank_mask:0xf
  s_waitcnt lgkmcnt(13)
  v_mfma_f32_16x16x32_bf16 v[116:119], v[40:43], v[192:195], v[116:119]
  v_max_f32_dpp v97, v97, v97 row_ror:4 row_mask:0xf bank_mask:0xf
  v_max_f32_dpp v98, v98, v98 row_ror:4 row_mask:0xf bank_mask:0xf
  v_max_f32_dpp v99, v99, v99 row_ror:4 row_mask:0xf bank_mask:0xf
  s_waitcnt lgkmcnt(12)
  v_mfma_f32_16x16x32_bf16 v[116:119], v[44:47], v[196:199], v[116:119]
  v_max_f32_dpp v96, v96, v96 row_ror:2 row_mask:0xf bank_mask:0xf
  v_max_f32_dpp v97, v97, v97 row_ror:2 row_mask:0xf bank_mask:0xf
  v_max_f32_dpp v98, v98, v98 row_ror:2 row_mask:0xf bank_mask:0xf
  s_waitcnt lgkmcnt(11)
  v_mfma_f32_16x16x32_bf16 v[120:123], v[48:51], v[176:179], 0
  v_max_f32_dpp v99, v99, v99 row_ror:2 row_mask:0xf bank_mask:0xf
  v_max_f32_dpp v96, v96, v96 row_ror:1 row_mask:0xf bank_mask:0xf
  v_max_f32_dpp v97, v97, v97 row_ror:1 row_mask:0xf bank_mask:0xf
  s_waitcnt lgkmcnt(10)
  v_mfma_f32_16x16x32_bf16 v[120:123], v[52:55], v[180:183], v[120:123]
  v_max_f32_dpp v98, v98, v98 row_ror:1 row_mask:0xf bank_mask:0xf
  v_max_f32_dpp v99, v99, v99 row_ror:1 row_mask:0xf bank_mask:0xf
  v_mul_f32 v116, v116, v247
  s_waitcnt lgkmcnt(9)
  v_mfma_f32_16x16x32_bf16 v[120:123], v[56:59], v[184:187], v[120:123]
  v_mul_f32 v117, v117, v247
  v_mul_f32 v118, v118, v247
  v_mul_f32 v119, v119, v247
  s_waitcnt lgkmcnt(8)
  v_mfma_f32_16x16x32_bf16 v[120:123], v[60:63], v[188:191], v[120:123]
  v_max_f32_dpp v100, v116, v116 row_ror:8 row_mask:0xf bank_mask:0xf
  v_max_f32_dpp v101, v117, v117 row_ror:8 row_mask:0xf bank_mask:0xf
  v_max_f32_dpp v102, v118, v118 row_ror:8 row_mask:0xf bank_mask:0xf
  s_waitcnt lgkmcnt(7)
  v_mfma_f32_16x16x32_bf16 v[120:123], v[64:67], v[192:195], v[120:123]
  v_max_f32_dpp v103, v119, v119 row_ror:8 row_mask:0xf bank_mask:0xf
  v_max_f32_dpp v100, v100, v100 row_ror:4 row_mask:0xf bank_mask:0xf
  v_max_f32_dpp v101, v101, v101 row_ror:4 row_mask:0xf bank_mask:0xf
  s_waitcnt lgkmcnt(6)
  v_mfma_f32_16x16x32_bf16 v[120:123], v[68:71], v[196:199], v[120:123]
  v_max_f32_dpp v102, v102, v102 row_ror:4 row_mask:0xf bank_mask:0xf
  v_max_f32_dpp v103, v103, v103 row_ror:4 row_mask:0xf bank_mask:0xf
  v_max_f32_dpp v100, v100, v100 row_ror:2 row_mask:0xf bank_mask:0xf
  s_waitcnt lgkmcnt(5)
  v_mfma_f32_16x16x32_bf16 v[124:127], v[72:75], v[176:179], 0
  v_max_f32_dpp v101, v101, v101 row_ror:2 row_mask:0xf bank_mask:0xf
  v_max_f32_dpp v102, v102, v102 row_ror:2 row_mask:0xf bank_mask:0xf
  v_max_f32_dpp v103, v103, v103 row_ror:2 row_mask:0xf bank_mask:0xf
  s_waitcnt lgkmcnt(4)
  v_mfma_f32_16x16x32_bf16 v[124:127], v[76:79], v[180:183], v[124:127]
  v_max_f32_dpp v100, v100, v100 row_ror:1 row_mask:0xf bank_mask:0xf
  v_max_f32_dpp v101, v101, v101 row_ror:1 row_mask:0xf bank_mask:0xf
  v_max_f32_dpp v102, v102, v102 row_ror:1 row_mask:0xf bank_mask:0xf
  s_waitcnt lgkmcnt(3)
  v_mfma_f32_16x16x32_bf16 v[124:127], v[80:83], v[184:187], v[124:127]
  v_max_f32_dpp v103, v103, v103 row_ror:1 row_mask:0xf bank_mask:0xf
  v_mul_f32 v120, v120, v247
  v_mul_f32 v121, v121, v247
  s_waitcnt lgkmcnt(2)
  v_mfma_f32_16x16x32_bf16 v[124:127], v[84:87], v[188:191], v[124:127]
  v_mul_f32 v122, v122, v247
  v_mul_f32 v123, v123, v247
  v_max_f32_dpp v104, v120, v120 row_ror:8 row_mask:0xf bank_mask:0xf
  s_waitcnt lgkmcnt(1)
  v_mfma_f32_16x16x32_bf16 v[124:127], v[88:91], v[192:195], v[124:127]
  v_max_f32_dpp v105, v121, v121 row_ror:8 row_mask:0xf bank_mask:0xf
  v_max_f32_dpp v106, v122, v122 row_ror:8 row_mask:0xf bank_mask:0xf
  v_max_f32_dpp v107, v123, v123 row_ror:8 row_mask:0xf bank_mask:0xf
  s_waitcnt lgkmcnt(0)
  v_mfma_f32_16x16x32_bf16 v[124:127], v[92:95], v[196:199], v[124:127]
  v_max_f32_dpp v104, v104, v104 row_ror:4 row_mask:0xf bank_mask:0xf
  v_max_f32_dpp v105, v105, v105 row_ror:4 row_mask:0xf bank_mask:0xf
  v_max_f32_dpp v106, v106, v106 row_ror:4 row_mask:0xf bank_mask:0xf
  v_max_f32_dpp v107, v107, v107 row_ror:4 row_mask:0xf bank_mask:0xf
  v_max_f32_dpp v104, v104, v104 row_ror:2 row_mask:0xf bank_mask:0xf
  v_max_f32_dpp v105, v105, v105 row_ror:2 row_mask:0xf bank_mask:0xf
  v_max_f32_dpp v106, v106, v106 row_ror:2 row_mask:0xf bank_mask:0xf
  v_max_f32_dpp v107, v107, v107 row_ror:2 row_mask:0xf bank_mask:0xf
  v_max_f32_dpp v104, v104, v104 row_ror:1 row_mask:0xf bank_mask:0xf
  v_max_f32_dpp v105, v105, v105 row_ror:1 row_mask:0xf bank_mask:0xf
  v_max_f32_dpp v106, v106, v106 row_ror:1 row_mask:0xf bank_mask:0xf
  v_max_f32_dpp v107, v107, v107 row_ror:1 row_mask:0xf bank_mask:0xf
  v_mul_f32 v124, v124, v247
  v_mul_f32 v125, v125, v247
  v_mul_f32 v126, v126, v247
  v_mul_f32 v127, v127, v247
  v_max_f32_dpp v108, v124, v124 row_ror:8 row_mask:0xf bank_mask:0xf
  v_max_f32_dpp v109, v125, v125 row_ror:8 row_mask:0xf bank_mask:0xf
  v_max_f32_dpp v110, v126, v126 row_ror:8 row_mask:0xf bank_mask:0xf
  v_max_f32_dpp v111, v127, v127 row_ror:8 row_mask:0xf bank_mask:0xf
  v_max_f32_dpp v108, v108, v108 row_ror:4 row_mask:0xf bank_mask:0xf
  v_max_f32_dpp v109, v109, v109 row_ror:4 row_mask:0xf bank_mask:0xf
  v_max_f32_dpp v110, v110, v110 row_ror:4 row_mask:0xf bank_mask:0xf
  v_max_f32_dpp v111, v111, v111 row_ror:4 row_mask:0xf bank_mask:0xf
  v_max_f32_dpp v108, v108, v108 row_ror:2 row_mask:0xf bank_mask:0xf
  v_max_f32_dpp v109, v109, v109 row_ror:2 row_mask:0xf bank_mask:0xf
  v_max_f32_dpp v110, v110, v110 row_ror:2 row_mask:0xf bank_mask:0xf
  v_max_f32_dpp v111, v111, v111 row_ror:2 row_mask:0xf bank_mask:0xf
  v_max_f32_dpp v108, v108, v108 row_ror:1 row_mask:0xf bank_mask:0xf
  v_max_f32_dpp v109, v109, v109 row_ror:1 row_mask:0xf bank_mask:0xf
  v_max_f32_dpp v110, v110, v110 row_ror:1 row_mask:0xf bank_mask:0xf
  v_max_f32_dpp v111, v111, v111 row_ror:1 row_mask:0xf bank_mask:0xf
  s_lshl_b32 s33, s27, 8
  v_add_u32 v254, s33, v255
  s_mov_b32 exec_lo, 0x00010001
  s_mov_b32 exec_hi, 0x00010001
  ds_write_b128 v254, v[96:99] offset:0
  ds_write_b128 v254, v[100:103] offset:16
  ds_write_b128 v254, v[104:107] offset:128
  ds_write_b128 v254, v[108:111] offset:144
  s_mov_b64 exec, -1
  s_waitcnt lgkmcnt(0)
  s_barrier
  ds_read2st64_b32 v[220:221], v251 offset0:0 offset1:1
  ds_read2st64_b32 v[222:223], v251 offset0:2 offset1:3
  s_waitcnt lgkmcnt(1)
  v_max_f32 v220, v220, v221
  s_waitcnt lgkmcnt(0)
  v_max3_f32 v220, v220, v222, v223
  v_ceil_f32_e64 v220, v220
  v_add_f32 v220, 0x42200000, v220
  v_max_f32 v220, 0xc9742400, v220
  v_min_f32 v220, 0x49742400, v220
  ds_write_b32 v251, v220 offset:1024
  ds_read_b128 v[96:99], v255 offset:1024
  ds_read_b128 v[100:103], v255 offset:1040
  ds_read_b128 v[104:107], v255 offset:1152
  ds_read_b128 v[108:111], v255 offset:1168
  s_waitcnt lgkmcnt(3)
  v_sub_f32 v112, v112, v96
  v_sub_f32 v113, v113, v97
  v_sub_f32 v114, v114, v98
  v_sub_f32 v115, v115, v99
  v_exp_f32 v220, v112
  v_exp_f32 v221, v113
  v_exp_f32 v222, v114
  v_exp_f32 v223, v115
  v_mul_f32 v220, v220, v247
  v_mul_f32 v221, v221, v247
  v_mul_f32 v222, v222, v247
  v_mul_f32_e64 v223, v223, v247
  v_cvt_pk_bf16_f32 v224, v220, v221
  v_cvt_pk_bf16_f32 v225, v222, v223
  s_waitcnt lgkmcnt(2)
  v_sub_f32 v116, v116, v100
  v_sub_f32 v117, v117, v101
  v_sub_f32 v118, v118, v102
  v_sub_f32 v119, v119, v103
  v_exp_f32 v212, v116
  v_exp_f32 v213, v117
  v_exp_f32 v214, v118
  v_exp_f32 v215, v119
  v_mul_f32 v212, v212, v247
  v_mul_f32 v213, v213, v247
  v_mul_f32 v214, v214, v247
  v_mul_f32_e64 v215, v215, v247
  v_cvt_pk_bf16_f32 v226, v212, v213
  v_cvt_pk_bf16_f32 v227, v214, v215
  ds_write_b128 v242, v[224:227] offset:0
  s_waitcnt lgkmcnt(2)
  v_sub_f32 v120, v120, v104
  v_sub_f32 v121, v121, v105
  v_sub_f32 v122, v122, v106
  v_sub_f32 v123, v123, v107
  v_exp_f32 v220, v120
  v_exp_f32 v221, v121
  v_exp_f32 v222, v122
  v_exp_f32 v223, v123
  v_mul_f32 v220, v220, v247
  v_mul_f32 v221, v221, v247
  v_mul_f32 v222, v222, v247
  v_mul_f32_e64 v223, v223, v247
  v_cvt_pk_bf16_f32 v228, v220, v221
  v_cvt_pk_bf16_f32 v229, v222, v223
  s_waitcnt lgkmcnt(1)
  v_sub_f32 v124, v124, v108
  v_sub_f32 v125, v125, v109
  v_sub_f32 v126, v126, v110
  v_sub_f32 v127, v127, v111
  v_exp_f32 v212, v124
  v_exp_f32 v213, v125
  v_exp_f32 v214, v126
  v_exp_f32 v215, v127
  v_mul_f32 v212, v212, v247
  v_mul_f32 v213, v213, v247
  v_mul_f32 v214, v214, v247
  v_mul_f32 v215, v215, v247
  v_cvt_pk_bf16_f32 v230, v212, v213
  v_cvt_pk_bf16_f32 v231, v214, v215
  ds_write_b128 v243, v[228:231] offset:0
  s_waitcnt lgkmcnt(0)
  s_barrier
  s_mov_b32 s32, 1
  s_mov_b32 s44, 1
.Lstop:
  s_setprio 2
  ds_read_b32 v251, v248
  s_and_b32 s34, s32, 3
  s_cbranch_scc0 .Lsc0
  ds_read_b128 v[176:179], v240 offset:0
  ds_read_b128 v[180:183], v241 offset:0
  ds_read_b128 v[184:187], v240 offset:128
  ds_read_b128 v[188:191], v241 offset:128
  ds_read_b128 v[192:195], v240 offset:256
  ds_read_b128 v[196:199], v241 offset:256
  s_movk_i32 s93, 6144
  s_cmp_eq_u32 s34, 3
  s_cmovk_i32 s93, -18432
  s_waitcnt lgkmcnt(6)
  v_readfirstlane_b32 s33, v251
  s_cmp_eq_u32 s33, s44
  s_cbranch_scc1 .Lsslow
  v_mov_b64_e64 v[216:217], 0
  s_waitcnt lgkmcnt(5)
  v_mfma_f32_4x4x4_16b_bf16 v[200:203], v[238:239], v[176:177], v[128:131]
  v_mfma_f32_4x4x4_16b_bf16 v[204:207], v[238:239], v[178:179], v[132:135]
  v_xor_b32 v244, 256, v244
  v_xor_b32 v248, 4, v248
  s_waitcnt lgkmcnt(4)
  v_mfma_f32_4x4x4_16b_bf16 v[208:211], v[238:239], v[180:181], v[136:139]
  v_mfma_f32_4x4x4_16b_bf16 v[220:223], v[238:239], v[182:183], v[140:143]
  s_waitcnt lgkmcnt(3)
  v_mfma_f32_4x4x4_16b_bf16 v[224:227], v[238:239], v[184:185], v[144:147]
  v_mfma_f32_4x4x4_16b_bf16 v[228:231], v[238:239], v[186:187], v[148:151]
  v_cvt_pk_bf16_f32 v176, v200, v201
  v_cvt_pk_bf16_f32 v177, v202, v203
  v_cvt_pk_bf16_f32 v178, v204, v205
  v_cvt_pk_bf16_f32 v179, v206, v207
  s_waitcnt lgkmcnt(2)
  v_mfma_f32_4x4x4_16b_bf16 v[200:203], v[238:239], v[188:189], v[152:155]
  v_mfma_f32_4x4x4_16b_bf16 v[204:207], v[238:239], v[190:191], v[156:159]
  v_mfma_f32_16x16x32_bf16 v[212:215], v[176:179], v[176:179], 0
  ds_write_b128 v240, v[176:179] offset:0
  v_cvt_pk_bf16_f32 v180, v208, v209
  v_cvt_pk_bf16_f32 v181, v210, v211
  v_cvt_pk_bf16_f32 v182, v220, v221
  v_cvt_pk_bf16_f32 v183, v222, v223
  s_waitcnt lgkmcnt(2)
  v_mfma_f32_4x4x4_16b_bf16 v[208:211], v[238:239], v[192:193], v[160:163]
  v_mfma_f32_4x4x4_16b_bf16 v[220:223], v[238:239], v[194:195], v[164:167]
  v_mfma_f32_16x16x32_bf16 v[212:215], v[180:183], v[180:183], v[212:215]
  v_smfmac_f32_16x16x64_bf16 v[216:219], v[232:235], v[176:183], v236
  ds_write_b128 v241, v[180:183] offset:0
  v_cvt_pk_bf16_f32 v184, v224, v225
  v_cvt_pk_bf16_f32 v185, v226, v227
  v_cvt_pk_bf16_f32 v186, v228, v229
  v_cvt_pk_bf16_f32 v187, v230, v231
  s_waitcnt lgkmcnt(2)
  v_mfma_f32_4x4x4_16b_bf16 v[224:227], v[238:239], v[196:197], v[168:171]
  v_mfma_f32_4x4x4_16b_bf16 v[228:231], v[238:239], v[198:199], v[172:175]
  v_mfma_f32_16x16x32_bf16 v[212:215], v[184:187], v[184:187], v[212:215]
  ds_write_b128 v240, v[184:187] offset:128
  v_cvt_pk_bf16_f32 v188, v200, v201
  v_cvt_pk_bf16_f32 v189, v202, v203
  v_cvt_pk_bf16_f32 v190, v204, v205
  v_cvt_pk_bf16_f32 v191, v206, v207
  s_nop 0
  s_nop 0
  v_mfma_f32_16x16x32_bf16 v[212:215], v[188:191], v[188:191], v[212:215]
  v_smfmac_f32_16x16x64_bf16 v[216:219], v[232:235], v[184:191], v236
  ds_write_b128 v241, v[188:191] offset:128
  v_cvt_pk_bf16_f32 v192, v208, v209
  v_cvt_pk_bf16_f32 v193, v210, v211
  v_cvt_pk_bf16_f32 v194, v220, v221
  v_cvt_pk_bf16_f32 v195, v222, v223
  s_nop 0
  s_nop 0
  v_mfma_f32_16x16x32_bf16 v[212:215], v[192:195], v[192:195], v[212:215]
  ds_write_b128 v240, v[192:195] offset:256
  v_cvt_pk_bf16_f32 v196, v224, v225
  v_cvt_pk_bf16_f32 v197, v226, v227
  v_cvt_pk_bf16_f32 v198, v228, v229
  v_cvt_pk_bf16_f32 v199, v230, v231
  s_nop 0
  s_nop 0
  v_mfma_f32_16x16x32_bf16 v[212:215], v[196:199], v[196:199], v[212:215]
  v_smfmac_f32_16x16x64_bf16 v[216:219], v[232:235], v[192:199], v236
  ds_write_b128 v241, v[196:199] offset:256
  v_xor_b32 v242, 0x2000, v242
  v_xor_b32 v243, 0x2000, v243
.Lsc1:
  v_mfma_f32_16x16x32_bf16 v[112:115], v[0:3], v[176:179], 0
  v_mfma_f32_16x16x32_bf16 v[112:115], v[4:7], v[180:183], v[112:115]
  v_add_u32_e64 v240, s93, v240
  v_add_u32 v241, s93, v241
  s_nop 0
  v_cndmask_b32 v252, v212, v213, s[48:49]
  v_mfma_f32_16x16x32_bf16 v[112:115], v[8:11], v[184:187], v[112:115]
  v_cndmask_b32 v253, v214, v215, s[48:49]
  v_add_f32_e64 v254, v216, v217
  v_cndmask_b32 v252, v252, v253, s[50:51]
  v_mfma_f32_16x16x32_bf16 v[112:115], v[12:15], v[188:191], v[112:115]
  s_setprio 0
  v_mul_f32 v254, v254, v254
  v_mov_b32_e64 v253, v252
  s_setprio 2
  v_mfma_f32_16x16x32_bf16 v[112:115], v[16:19], v[192:195], v[112:115]
  v_permlane32_swap_b32 v252, v253
  v_cndmask_b32 v252, v252, v253, s[56:57]
  v_mov_b32 v253, v252
  v_mfma_f32_16x16x32_bf16 v[112:115], v[20:23], v[196:199], v[112:115]
  v_mfma_f32_16x16x32_bf16 v[116:119], v[24:27], v[176:179], 0
  v_permlane16_swap_b32 v252, v253
  v_cndmask_b32 v252, v252, v253, s[52:53]
  v_fma_f32 v252, -v254, v249, v252
  v_mfma_f32_16x16x32_bf16 v[116:119], v[28:31], v[180:183], v[116:119]
  s_setprio 0
  v_max_f32 v252, 0, v252
  v_fmaak_f32 v252, v249, v252, 0x3727c5ac
  v_rsq_f32 v247, v252
  s_setprio 2
  v_mfma_f32_16x16x32_bf16 v[116:119], v[32:35], v[184:187], v[116:119]
  v_fma_f32 v112, v112, v247, -v96
  v_mul_f32_e64 v253, v252, v247
  v_and_b32 v254, 0xffff0000, v253
  v_mfma_f32_16x16x32_bf16 v[116:119], v[36:39], v[188:191], v[116:119]
  ds_write_b16_d16_hi v244, v253 offset:0
  v_sub_f32_e64 v254, v253, v254
  v_cvt_pk_bf16_f32 v254, v254, v254
  v_mfma_f32_16x16x32_bf16 v[116:119], v[40:43], v[192:195], v[116:119]
  ds_write_b16 v244, v254 offset:128
  v_fma_f32 v113, v113, v247, -v97
  v_fma_f32 v114, v114, v247, -v98
  v_mfma_f32_16x16x32_bf16 v[116:119], v[44:47], v[196:199], v[116:119]
  s_setprio 0
  v_fma_f32 v115, v115, v247, -v99
  v_exp_f32 v220, v112
  s_setprio 2
  v_mfma_f32_16x16x32_bf16 v[120:123], v[48:51], v[176:179], 0
  v_exp_f32 v221, v113
  v_exp_f32_e64 v222, v114
  v_mfma_f32_16x16x32_bf16 v[120:123], v[52:55], v[180:183], v[120:123]
  v_exp_f32 v223, v115
  v_mul_f32 v220, v220, v247
  v_mfma_f32_16x16x32_bf16 v[120:123], v[56:59], v[184:187], v[120:123]
  v_mul_f32 v221, v221, v247
  v_mul_f32 v222, v222, v247
  v_mul_f32_e64 v223, v223, v247
  v_mfma_f32_16x16x32_bf16 v[120:123], v[60:63], v[188:191], v[120:123]
  s_setprio 0
  v_cvt_pk_bf16_f32 v224, v220, v221
  v_cvt_pk_bf16_f32 v225, v222, v223
  v_fma_f32 v116, v116, v247, -v100
  s_setprio 2
  v_mfma_f32_16x16x32_bf16 v[120:123], v[64:67], v[192:195], v[120:123]
  v_fma_f32 v117, v117, v247, -v101
  v_fma_f32 v118, v118, v247, -v102
  v_fma_f32 v119, v119, v247, -v103
  v_mfma_f32_16x16x32_bf16 v[120:123], v[68:71], v[196:199], v[120:123]
  v_exp_f32 v212, v116
  v_exp_f32 v213, v117
  v_mfma_f32_16x16x32_bf16 v[124:127], v[72:75], v[176:179], 0
  v_exp_f32 v214, v118
  v_exp_f32 v215, v119
  v_mfma_f32_16x16x32_bf16 v[124:127], v[76:79], v[180:183], v[124:127]
  s_setprio 0
  v_mul_f32 v212, v212, v247
  v_mul_f32_e64 v213, v213, v247
  v_mul_f32 v214, v214, v247
  s_setprio 2
  v_mfma_f32_16x16x32_bf16 v[124:127], v[80:83], v[184:187], v[124:127]
  v_mul_f32_e64 v215, v215, v247
  v_cvt_pk_bf16_f32 v226, v212, v213
  v_cvt_pk_bf16_f32 v227, v214, v215
  v_mfma_f32_16x16x32_bf16 v[124:127], v[84:87], v[188:191], v[124:127]
  ds_write_b128 v242, v[224:227] offset:0
  v_fma_f32 v120, v120, v247, -v104
  v_fma_f32 v121, v121, v247, -v105
  v_mfma_f32_16x16x32_bf16 v[124:127], v[88:91], v[192:195], v[124:127]
  v_fma_f32 v122, v122, v247, -v106
  v_fma_f32 v123, v123, v247, -v107
  v_exp_f32_e64 v220, v120
  v_mfma_f32_16x16x32_bf16 v[124:127], v[92:95], v[196:199], v[124:127]
  s_setprio 0
  v_exp_f32 v221, v121
  v_exp_f32 v222, v122
  v_exp_f32 v223, v123
  v_mul_f32 v220, v220, v247
  v_mul_f32 v221, v221, v247
  v_mul_f32 v222, v222, v247
  v_mul_f32 v223, v223, v247
  v_cvt_pk_bf16_f32 v228, v220, v221
  v_cvt_pk_bf16_f32 v229, v222, v223
  v_fma_f32 v124, v124, v247, -v108
  v_fma_f32 v125, v125, v247, -v109
  v_fma_f32 v126, v126, v247, -v110
  v_fma_f32 v127, v127, v247, -v111
  v_exp_f32 v212, v124
  v_exp_f32 v213, v125
  v_exp_f32 v214, v126
  v_exp_f32 v215, v127
  v_mul_f32 v212, v212, v247
  v_mul_f32 v213, v213, v247
  v_mul_f32 v214, v214, v247
  v_mul_f32 v215, v215, v247
  v_cvt_pk_bf16_f32 v230, v212, v213
  v_cvt_pk_bf16_f32 v231, v214, v215
  ds_write_b128 v243, v[228:231] offset:0
.Lsend:
  v_or3_b32 v252, v224, v225, v226
  v_or3_b32 v253, v227, v228, v229
  v_or3_b32 v252, v252, v230, v231
  v_or_b32_e64 v252, v252, v253
  v_and_b32 v252, 0x40004000, v252
  v_cmp_ne_u32_e64 vcc, 0, v252
  s_add_u32 s44, s32, 1
  s_cbranch_vccnz .Lsalarm
.Lsend2:
  s_waitcnt lgkmcnt(0)
  s_barrier
  s_mov_b32 s32, s44
  s_branch .Lstop
.Lsalarm:
  v_mov_b32 v252, s44
  ds_write_b32 v248, v252
  s_branch .Lsend2
.Lsc0:
  s_cmp_eq_u32 s32, 16
  s_cbranch_scc1 .Lsfin
  ds_read_b64_tr_b16 v[152:153], v245 offset:0
  ds_read_b64_tr_b16 v[154:155], v246 offset:0
  ds_read_b128 v[176:179], v240 offset:0
  ds_read_b64_tr_b16 v[156:157], v245 offset:256
  ds_read_b64_tr_b16 v[158:159], v246 offset:256
  ds_read_b128 v[180:183], v241 offset:0
  ds_read_b64_tr_b16 v[160:161], v245 offset:2048
  ds_read_b64_tr_b16 v[162:163], v246 offset:2048
  ds_read_b128 v[184:187], v240 offset:128
  ds_read_b64_tr_b16 v[164:165], v245 offset:2304
  ds_read_b64_tr_b16 v[166:167], v246 offset:2304
  ds_read_b128 v[188:191], v241 offset:128
  ds_read_b64_tr_b16 v[168:169], v245 offset:4096
  ds_read_b64_tr_b16 v[170:171], v246 offset:4096
  s_waitcnt lgkmcnt(14)
  v_readfirstlane_b32 s33, v251
  s_cmp_eq_u32 s33, s44
  s_cbranch_scc1 .Lsslow
  ds_read_b128 v[192:195], v240 offset:256
  ds_read_b64_tr_b16 v[172:173], v245 offset:4352
  ds_read_b64_tr_b16 v[124:125], v246 offset:4352
  ds_read_b128 v[196:199], v241 offset:256
  s_movk_i32 s93, 6144
  v_mov_b64 v[216:217], 0
  s_waitcnt lgkmcnt(15)
  v_mfma_f32_4x4x4_16b_bf16 v[128:131], v[238:239], v[152:153], 0
  v_mfma_f32_4x4x4_16b_bf16 v[132:135], v[238:239], v[154:155], 0
  s_nop 0
  v_mfma_f32_4x4x4_16b_bf16 v[200:203], v[238:239], v[176:177], v[128:131]
  v_mfma_f32_4x4x4_16b_bf16 v[204:207], v[238:239], v[178:179], v[132:135]
  s_waitcnt lgkmcnt(14)
  v_mfma_f32_4x4x4_16b_bf16 v[136:139], v[238:239], v[156:157], 0
  s_waitcnt lgkmcnt(13)
  v_mfma_f32_4x4x4_16b_bf16 v[140:143], v[238:239], v[158:159], 0
  v_xor_b32 v244, 256, v244
  v_xor_b32 v248, 4, v248
  s_waitcnt lgkmcnt(12)
  v_mfma_f32_4x4x4_16b_bf16 v[208:211], v[238:239], v[180:181], v[136:139]
  v_mfma_f32_4x4x4_16b_bf16 v[220:223], v[238:239], v[182:183], v[140:143]
  s_waitcnt lgkmcnt(11)
  v_mfma_f32_4x4x4_16b_bf16 v[144:147], v[238:239], v[160:161], 0
  s_waitcnt lgkmcnt(10)
  v_mfma_f32_4x4x4_16b_bf16 v[148:151], v[238:239], v[162:163], 0
  v_xor_b32 v242, 0x2000, v242
  v_xor_b32 v243, 0x2000, v243
  s_waitcnt lgkmcnt(9)
  v_mfma_f32_4x4x4_16b_bf16 v[224:227], v[238:239], v[184:185], v[144:147]
  v_mfma_f32_4x4x4_16b_bf16 v[228:231], v[238:239], v[186:187], v[148:151]
  v_cvt_pk_bf16_f32 v176, v200, v201
  v_cvt_pk_bf16_f32 v177, v202, v203
  v_cvt_pk_bf16_f32 v178, v204, v205
  v_cvt_pk_bf16_f32 v179, v206, v207
  s_waitcnt lgkmcnt(8)
  v_mfma_f32_4x4x4_16b_bf16 v[152:155], v[238:239], v[164:165], 0
  s_waitcnt lgkmcnt(7)
  v_mfma_f32_4x4x4_16b_bf16 v[156:159], v[238:239], v[166:167], 0
  s_waitcnt lgkmcnt(6)
  v_mfma_f32_4x4x4_16b_bf16 v[200:203], v[238:239], v[188:189], v[152:155]
  v_mfma_f32_4x4x4_16b_bf16 v[204:207], v[238:239], v[190:191], v[156:159]
  v_mfma_f32_16x16x32_bf16 v[212:215], v[176:179], v[176:179], 0
  ds_write_b128 v240, v[176:179] offset:0
  v_cvt_pk_bf16_f32 v180, v208, v209
  v_cvt_pk_bf16_f32 v181, v210, v211
  v_cvt_pk_bf16_f32 v182, v220, v221
  v_cvt_pk_bf16_f32 v183, v222, v223
  s_waitcnt lgkmcnt(6)
  v_mfma_f32_4x4x4_16b_bf16 v[160:163], v[238:239], v[168:169], 0
  s_waitcnt lgkmcnt(5)
  v_mfma_f32_4x4x4_16b_bf16 v[164:167], v[238:239], v[170:171], 0
  s_waitcnt lgkmcnt(4)
  v_mfma_f32_4x4x4_16b_bf16 v[208:211], v[238:239], v[192:193], v[160:163]
  v_mfma_f32_4x4x4_16b_bf16 v[220:223], v[238:239], v[194:195], v[164:167]
  v_mfma_f32_16x16x32_bf16 v[212:215], v[180:183], v[180:183], v[212:215]
  v_smfmac_f32_16x16x64_bf16 v[216:219], v[232:235], v[176:183], v236
  ds_write_b128 v241, v[180:183] offset:0
  v_cvt_pk_bf16_f32 v184, v224, v225
  v_cvt_pk_bf16_f32 v185, v226, v227
  v_cvt_pk_bf16_f32 v186, v228, v229
  v_cvt_pk_bf16_f32 v187, v230, v231
  s_waitcnt lgkmcnt(4)
  v_mfma_f32_4x4x4_16b_bf16 v[168:171], v[238:239], v[172:173], 0
  s_waitcnt lgkmcnt(3)
  v_mfma_f32_4x4x4_16b_bf16 v[172:175], v[238:239], v[124:125], 0
  s_waitcnt lgkmcnt(2)
  v_mfma_f32_4x4x4_16b_bf16 v[224:227], v[238:239], v[196:197], v[168:171]
  v_mfma_f32_4x4x4_16b_bf16 v[228:231], v[238:239], v[198:199], v[172:175]
  v_mfma_f32_16x16x32_bf16 v[212:215], v[184:187], v[184:187], v[212:215]
  ds_write_b128 v240, v[184:187] offset:128
  v_cvt_pk_bf16_f32 v188, v200, v201
  v_cvt_pk_bf16_f32 v189, v202, v203
  v_cvt_pk_bf16_f32 v190, v204, v205
  v_cvt_pk_bf16_f32 v191, v206, v207
  s_nop 0
  s_nop 0
  v_mfma_f32_16x16x32_bf16 v[212:215], v[188:191], v[188:191], v[212:215]
  v_smfmac_f32_16x16x64_bf16 v[216:219], v[232:235], v[184:191], v236
  ds_write_b128 v241, v[188:191] offset:128
  v_cvt_pk_bf16_f32 v192, v208, v209
  v_cvt_pk_bf16_f32 v193, v210, v211
  v_cvt_pk_bf16_f32 v194, v220, v221
  v_cvt_pk_bf16_f32 v195, v222, v223
  s_nop 0
  s_nop 0
  v_mfma_f32_16x16x32_bf16 v[212:215], v[192:195], v[192:195], v[212:215]
  ds_write_b128 v240, v[192:195] offset:256
  v_cvt_pk_bf16_f32 v196, v224, v225
  v_cvt_pk_bf16_f32 v197, v226, v227
  v_cvt_pk_bf16_f32 v198, v228, v229
  v_cvt_pk_bf16_f32 v199, v230, v231
  s_nop 1
  v_mfma_f32_16x16x32_bf16 v[212:215], v[196:199], v[196:199], v[212:215]
  v_smfmac_f32_16x16x64_bf16 v[216:219], v[232:235], v[192:199], v236
  ds_write_b128 v241, v[196:199] offset:256
  s_branch .Lsc1
.Lsfin:
  s_waitcnt lgkmcnt(0)
  v_readfirstlane_b32 s33, v251
  s_cmp_eq_u32 s33, s44
  s_cbranch_scc1 .Lsslow
  v_mbcnt_lo_u32_b32 v60, -1, 0
  v_mbcnt_hi_u32_b32 v60, -1, v60
  s_lshr_b32 s33, s2, 6
  s_and_b32 s34, s2, 3
  s_mul_i32 s35, s33, 18432
  s_mul_i32 s36, s34, 1536
  s_add_u32 s35, s35, s36
  s_mul_i32 s36, s27, 96
  s_add_u32 s35, s35, s36
  v_lshl_add_u32 v62, v60, 2, s35
  s_mov_b64 exec, 0xffffff
  global_load_dword v16, v62, s[18:19] offset:0
  global_load_dword v17, v62, s[18:19] offset:384
  global_load_dword v18, v62, s[18:19] offset:768
  global_load_dword v19, v62, s[18:19] offset:1152
  s_add_u32 s18, s18, 0x1800
  s_addc_u32 s19, s19, 0x0
  global_load_dword v20, v62, s[18:19] offset:0
  global_load_dword v21, v62, s[18:19] offset:384
  global_load_dword v22, v62, s[18:19] offset:768
  global_load_dword v23, v62, s[18:19] offset:1152
  s_add_u32 s18, s18, 0x1800
  s_addc_u32 s19, s19, 0x0
  global_load_dword v24, v62, s[18:19] offset:0
  global_load_dword v25, v62, s[18:19] offset:384
  global_load_dword v26, v62, s[18:19] offset:768
  global_load_dword v27, v62, s[18:19] offset:1152
  s_mov_b64 exec, -1
  s_lshl_b32 s35, s2, 14
  s_lshl_b32 s36, s27, 12
  s_add_u32 s35, s35, s36
  s_add_u32 s35, s35, 65536
  v_lshl_add_u32 v61, v60, 4, s35
  s_lshl_b32 s33, s33, 2
  s_add_u32 s33, s33, s34
  s_mul_i32 s33, s33, 1536
  s_lshr_b32 s34, s2, 2
  s_and_b32 s34, s34, 15
  s_mul_i32 s34, s34, 24576
  s_add_u32 s33, s33, s34
  s_mul_i32 s34, s27, 384
  s_add_u32 s33, s33, s34
  s_add_u32 s33, s33, 4259840
  v_lshl_add_u32 v63, v60, 4, s33
  v_lshlrev_b32_e64 v64, 3, v60
  v_add_u32 v64, 98304, v64
  v_and_b32 v72, 15, v60
  v_lshrrev_b32 v73, 4, v60
  s_and_b32 s33, s2, 15
  s_lshr_b32 s34, s2, 4
  s_mul_i32 s35, s33, 24576
  s_lshl_b32 s36, s34, 8
  s_add_u32 s35, s35, s36
  s_add_u32 s35, s35, 4259840
  v_mul_u32_u24 v66, 1536, v73
  v_lshl_add_u32 v66, v72, 4, v66
  v_add_u32_e64 v66, s35, v66
  v_add_u32 v67, 6144, v66
  v_add_u32 v68, 12288, v66
  v_add_u32 v69, 18432, v66
  s_mul_i32 s33, s33, 384
  s_lshl_b32 s34, s34, 6
  s_add_u32 s33, s33, s34
  v_lshrrev_b32_e64 v73, 1, v73
  v_lshl_add_u32 v72, v72, 1, v73
  v_lshl_add_u32 v70, v72, 1, s33
  s_cmp_lt_u32 s2, 96
  s_cbranch_scc0 .Lnoq
  global_load_ushort v65, v70, s[4:5]
.Lnoq:
  s_waitcnt lgkmcnt(0)
  s_barrier
  s_waitcnt vmcnt(11)
  v_lshlrev_b32_e64 v32, 16, v16
  v_and_b32 v33, 0xffff0000, v16
  s_waitcnt vmcnt(10)
  v_lshlrev_b32 v34, 16, v17
  v_and_b32 v35, 0xffff0000, v17
  s_waitcnt vmcnt(9)
  v_lshlrev_b32 v36, 16, v18
  v_and_b32 v37, 0xffff0000, v18
  s_waitcnt vmcnt(8)
  v_lshlrev_b32 v38, 16, v19
  v_and_b32 v39, 0xffff0000, v19
  s_waitcnt vmcnt(7)
  v_lshlrev_b32 v40, 16, v20
  v_and_b32 v41, 0xffff0000, v20
  s_waitcnt vmcnt(6)
  v_lshlrev_b32 v42, 16, v21
  v_and_b32 v43, 0xffff0000, v21
  s_waitcnt vmcnt(5)
  v_lshlrev_b32 v44, 16, v22
  v_and_b32 v45, 0xffff0000, v22
  s_waitcnt vmcnt(4)
  v_lshlrev_b32 v46, 16, v23
  v_and_b32 v47, 0xffff0000, v23
  s_waitcnt vmcnt(3)
  v_lshlrev_b32 v48, 16, v24
  v_and_b32 v49, 0xffff0000, v24
  s_waitcnt vmcnt(2)
  v_lshlrev_b32 v50, 16, v25
  v_and_b32 v51, 0xffff0000, v25
  s_waitcnt vmcnt(1)
  v_lshlrev_b32 v52, 16, v26
  v_and_b32 v53, 0xffff0000, v26
  s_waitcnt vmcnt(0)
  v_lshlrev_b32 v54, 16, v27
  v_and_b32 v55, 0xffff0000, v27
  s_mov_b32 s33, 0
  s_mov_b32 s35, 0x07060302
  s_lshl_b32 s36, s27, 9
  v_add_u32 v72, s36, v64
  s_barrier
.Lg1:
  global_load_dwordx4 v[0:3], v61, s[24:25] offset:0 sc1
  global_load_dwordx4 v[4:7], v61, s[24:25] offset:1024 sc1
  global_load_dwordx4 v[8:11], v61, s[24:25] offset:2048 sc1
  global_load_dwordx4 v[12:15], v61, s[24:25] offset:3072 sc1
  s_waitcnt vmcnt(3)
  v_bitop3_b32 v16, v1, v3, s54 bitop3:0x7e
  s_waitcnt vmcnt(2)
  v_bitop3_b32 v17, v5, v7, s54 bitop3:0x7e
  s_waitcnt vmcnt(1)
  v_bitop3_b32 v18, v9, v11, s54 bitop3:0x7e
  s_waitcnt vmcnt(0)
  v_bitop3_b32 v19, v13, v15, s54 bitop3:0x7e
  v_or3_b32 v16, v16, v17, v18
  v_or_b32_e64 v16, v16, v19
  v_cmp_ne_u32 vcc, 0, v16
  s_add_u32 s33, s33, 1
  s_cbranch_vccz .Lg1d
  s_mov_b64 exec, vcc
  s_cmp_lt_u32 s33, 32768
  s_cbranch_scc1 .Lg1
.Lg1d:
  s_mov_b64 exec, -1
  v_mov_b32_dpp v16, v2 quad_perm:[2,2,2,2] row_mask:0xf bank_mask:0xf
  v_mov_b32_dpp v24, v2 quad_perm:[3,3,3,3] row_mask:0xf bank_mask:0xf
  v_mov_b32_dpp v17, v6 quad_perm:[2,2,2,2] row_mask:0xf bank_mask:0xf
  v_mov_b32_dpp v25, v6 quad_perm:[3,3,3,3] row_mask:0xf bank_mask:0xf
  v_mov_b32_dpp v18, v10 quad_perm:[2,2,2,2] row_mask:0xf bank_mask:0xf
  v_mov_b32_dpp v26, v10 quad_perm:[3,3,3,3] row_mask:0xf bank_mask:0xf
  v_mov_b32_dpp v19, v14 quad_perm:[2,2,2,2] row_mask:0xf bank_mask:0xf
  v_mov_b32_dpp v27, v14 quad_perm:[3,3,3,3] row_mask:0xf bank_mask:0xf
  v_perm_b32 v16, v16, v24, s35
  v_perm_b32 v17, v17, v25, s35
  v_perm_b32 v18, v18, v26, s35
  v_perm_b32 v19, v19, v27, s35
  v_max3_f32 v28, v16, v17, v18
  v_max_f32_e64 v28, v28, v19
  v_mov_b32_dpp v20, v2 quad_perm:[0,0,0,0] row_mask:0xf bank_mask:0xf
  v_mov_b32_dpp v24, v2 quad_perm:[1,1,1,1] row_mask:0xf bank_mask:0xf
  v_mov_b32_dpp v21, v6 quad_perm:[0,0,0,0] row_mask:0xf bank_mask:0xf
  v_max_f32_dpp v28, v28, v28 row_ror:4 row_mask:0xf bank_mask:0xf
  v_mov_b32_dpp v25, v6 quad_perm:[1,1,1,1] row_mask:0xf bank_mask:0xf
  v_mov_b32_dpp v22, v10 quad_perm:[0,0,0,0] row_mask:0xf bank_mask:0xf
  v_mov_b32_dpp v26, v10 quad_perm:[1,1,1,1] row_mask:0xf bank_mask:0xf
  v_max_f32_dpp v28, v28, v28 row_ror:8 row_mask:0xf bank_mask:0xf
  v_mov_b32_dpp v23, v14 quad_perm:[0,0,0,0] row_mask:0xf bank_mask:0xf
  v_mov_b32_dpp v27, v14 quad_perm:[1,1,1,1] row_mask:0xf bank_mask:0xf
  v_perm_b32 v20, v20, v24, s35
  v_max_f32_dpp v28, v28, v28 row_bcast:15 row_mask:0xa bank_mask:0xf
  v_perm_b32 v21, v21, v25, s35
  v_perm_b32 v22, v22, v26, s35
  v_perm_b32 v23, v23, v27, s35
  v_max_f32_dpp v28, v28, v28 row_bcast:31 row_mask:0xc bank_mask:0xf
  v_lshlrev_b32_e64 v1, 16, v0
  v_and_b32 v0, 0xffff0000, v0
  v_lshlrev_b32_e64 v2, 16, v2
  v_readlane_b32 s34, v28, 63
  v_lshlrev_b32_e64 v5, 16, v4
  v_and_b32 v4, 0xffff0000, v4
  v_lshlrev_b32 v6, 16, v6
  v_lshlrev_b32 v9, 16, v8
  v_and_b32 v8, 0xffff0000, v8
  v_lshlrev_b32 v10, 16, v10
  v_lshlrev_b32 v13, 16, v12
  v_and_b32 v12, 0xffff0000, v12
  v_lshlrev_b32 v14, 16, v14
  v_subrev_f32 v16, s34, v16
  v_subrev_f32 v17, s34, v17
  v_subrev_f32 v18, s34, v18
  v_subrev_f32 v19, s34, v19
  v_exp_f32 v16, v16
  v_exp_f32 v17, v17
  v_exp_f32 v18, v18
  v_exp_f32 v19, v19
  v_mul_f32 v24, v16, v1
  v_mul_f32 v25, v16, v0
  v_mul_f32 v26, v16, v2
  v_mul_f32 v27, v16, v20
  v_fmac_f32 v24, v17, v5
  v_fmac_f32 v25, v17, v4
  v_fmac_f32 v26, v17, v6
  v_fmac_f32 v27, v17, v21
  v_fmac_f32 v24, v18, v9
  v_fmac_f32 v25, v18, v8
  v_fmac_f32 v26, v18, v10
  v_fmac_f32 v27, v18, v22
  v_fmac_f32 v24, v19, v13
  v_fmac_f32 v25, v19, v12
  v_fmac_f32 v26, v19, v14
  v_fmac_f32 v27, v19, v23
  v_mov_b32 v29, s34
  v_permlane32_swap_b32 v24, v25
  v_permlane32_swap_b32 v26, v27
  v_add_f32 v24, v24, v25
  v_add_f32 v26, v26, v27
  s_nop 1
  v_permlane16_swap_b32 v24, v26
  v_add_f32 v28, v24, v26
  s_nop 1
  v_add_f32_dpp v28, v28, v28 row_ror:4 row_mask:0xf bank_mask:0xf
  s_nop 0
  s_nop 0
  v_add_f32_dpp v28, v28, v28 row_ror:8 row_mask:0xf bank_mask:0xf
  ds_write_b64 v72, v[28:29]
  s_waitcnt lgkmcnt(0)
  s_barrier
  ds_read_b64 v[0:1], v64 offset:0
  ds_read_b64 v[2:3], v64 offset:512
  ds_read_b64 v[4:5], v64 offset:1024
  ds_read_b64 v[6:7], v64 offset:1536
  s_cmp_eq_u32 s2, 0
  s_cbranch_scc0 .Lnox
  v_mov_b32 v16, 61440
  v_mov_b32 v17, s54
  s_mov_b64 exec, 1
  global_store_dword v16, v17, s[24:25] sc1
  s_mov_b64 exec, -1
.Lnox:
  s_waitcnt lgkmcnt(1)
  v_max3_f32 v8, v1, v3, v5
  s_waitcnt lgkmcnt(0)
  v_max_f32 v8, v8, v7
  v_sub_f32 v1, v1, v8
  v_sub_f32 v3, v3, v8
  v_sub_f32 v5, v5, v8
  v_sub_f32 v7, v7, v8
  v_exp_f32 v1, v1
  v_exp_f32 v3, v3
  v_exp_f32 v5, v5
  v_exp_f32 v7, v7
  v_mul_f32 v8, v0, v1
  v_fmac_f32 v8, v2, v3
  v_fmac_f32_e64 v8, v4, v5
  v_fmac_f32 v8, v6, v7
  s_nop 0
  v_readlane_b32 s33, v8, 48
  s_nop 1
  v_rcp_f32 v9, s33
  v_mov_b32 v1, s54
  v_mov_b32 v3, s54
  v_mul_f32 v8, v8, v9
  s_nop 0
  v_readlane_b32 s6, v8, 0
  v_readlane_b32 s7, v8, 1
  v_readlane_b32 s8, v8, 2
  v_readlane_b32 s9, v8, 3
  v_mul_f32 v0, s6, v32
  v_mul_f32 v2, s6, v33
  v_readlane_b32 s10, v8, 32
  v_fmac_f32 v0, s7, v34
  v_fmac_f32 v2, s7, v35
  v_readlane_b32 s11, v8, 33
  v_fmac_f32 v0, s8, v36
  v_fmac_f32 v2, s8, v37
  v_readlane_b32 s12, v8, 34
  v_fmac_f32 v0, s9, v38
  v_fmac_f32 v2, s9, v39
  v_readlane_b32 s13, v8, 35
  v_fmac_f32 v0, s10, v40
  v_fmac_f32 v2, s10, v41
  v_readlane_b32 s14, v8, 16
  v_fmac_f32 v0, s11, v42
  v_fmac_f32 v2, s11, v43
  v_readlane_b32 s15, v8, 17
  v_fmac_f32 v0, s12, v44
  v_fmac_f32 v2, s12, v45
  v_readlane_b32 s16, v8, 18
  v_fmac_f32 v0, s13, v46
  v_fmac_f32 v2, s13, v47
  v_readlane_b32 s17, v8, 19
  v_fmac_f32 v0, s14, v48
  v_fmac_f32 v2, s14, v49
  v_fmac_f32 v0, s15, v50
  v_fmac_f32 v2, s15, v51
  v_fmac_f32 v0, s16, v52
  v_fmac_f32 v2, s16, v53
  v_fmac_f32 v0, s17, v54
  v_fmac_f32 v2, s17, v55
  s_mov_b64 exec, 0xffffff
  global_store_dwordx4 v63, v[0:3], s[24:25] sc1
  s_mov_b64 exec, -1
  s_cmp_lt_u32 s2, 96
  s_cselect_b32 s33, s27, 1
  s_cmp_eq_u32 s33, 0
  s_cbranch_scc0 .Lsdone
  v_lshlrev_b32_e64 v65, 16, v65
.Lg2:
  global_load_dwordx4 v[0:3], v66, s[24:25] offset:0 sc1
  global_load_dwordx4 v[4:7], v67, s[24:25] offset:0 sc1
  global_load_dwordx4 v[8:11], v68, s[24:25] offset:0 sc1
  global_load_dwordx4 v[12:15], v69, s[24:25] offset:0 sc1
  s_waitcnt vmcnt(3)
  v_bitop3_b32 v16, v1, v3, s54 bitop3:0x7e
  s_waitcnt vmcnt(2)
  v_bitop3_b32 v17, v5, v7, s54 bitop3:0x7e
  s_waitcnt vmcnt(1)
  v_bitop3_b32 v18, v9, v11, s54 bitop3:0x7e
  s_waitcnt vmcnt(0)
  v_bitop3_b32 v19, v13, v15, s54 bitop3:0x7e
  v_or3_b32 v16, v16, v17, v18
  v_or_b32_e64 v16, v16, v19
  v_cmp_ne_u32 vcc, 0, v16
  s_add_u32 s33, s33, 1
  s_cbranch_vccz .Lg2d
  s_mov_b64 exec, vcc
  s_cmp_lt_u32 s33, 32768
  s_cbranch_scc1 .Lg2
.Lg2d:
  s_mov_b64 exec, -1
  v_add_f32 v0, v0, v4
  v_add_f32 v2, v2, v6
  v_add_f32 v8, v8, v12
  v_add_f32 v10, v10, v14
  v_add_f32 v0, v0, v8
  v_add_f32 v2, v2, v10
  s_nop 1
  v_permlane32_swap_b32 v0, v2
  v_add_f32 v0, v0, v2
  v_mov_b32 v2, v0
  s_nop 1
  v_permlane16_swap_b32 v0, v2
  v_add_f32 v0, v0, v2
  v_add_f32 v0, v0, v65
  v_cvt_pk_bf16_f32 v0, v0, v0
  s_mov_b32 exec_lo, 0xffff
  s_mov_b32 exec_hi, 0xffff
  global_store_short v70, v0, s[22:23]
  s_mov_b64 exec, -1
.Lsdone:
  s_endpgm
.Lsslow:
  v_mbcnt_lo_u32_b32 v251, -1, 0
  v_mbcnt_hi_u32_b32 v251, -1, v251
  v_lshrrev_b32 v255, 4, v251
  v_lshlrev_b32 v251, 2, v251
  v_lshlrev_b32_e64 v255, 5, v255
  v_add_u32 v251, 158208, v251
  v_add_u32 v255, 158208, v255
  s_waitcnt lgkmcnt(5)
  v_max_f32_dpp v176, v112, v112 row_ror:8 row_mask:0xf bank_mask:0xf
  v_max_f32_dpp v177, v113, v113 row_ror:8 row_mask:0xf bank_mask:0xf
  v_max_f32_dpp v178, v114, v114 row_ror:8 row_mask:0xf bank_mask:0xf
  v_max_f32_dpp v179, v115, v115 row_ror:8 row_mask:0xf bank_mask:0xf
  v_max_f32_dpp v176, v176, v176 row_ror:4 row_mask:0xf bank_mask:0xf
  v_max_f32_dpp v177, v177, v177 row_ror:4 row_mask:0xf bank_mask:0xf
  v_max_f32_dpp v178, v178, v178 row_ror:4 row_mask:0xf bank_mask:0xf
  v_max_f32_dpp v179, v179, v179 row_ror:4 row_mask:0xf bank_mask:0xf
  v_max_f32_dpp v176, v176, v176 row_ror:2 row_mask:0xf bank_mask:0xf
  v_max_f32_dpp v177, v177, v177 row_ror:2 row_mask:0xf bank_mask:0xf
  v_max_f32_dpp v178, v178, v178 row_ror:2 row_mask:0xf bank_mask:0xf
  v_max_f32_dpp v179, v179, v179 row_ror:2 row_mask:0xf bank_mask:0xf
  v_max_f32_dpp v176, v176, v176 row_ror:1 row_mask:0xf bank_mask:0xf
  v_max_f32_dpp v177, v177, v177 row_ror:1 row_mask:0xf bank_mask:0xf
  v_max_f32_dpp v178, v178, v178 row_ror:1 row_mask:0xf bank_mask:0xf
  v_max_f32_dpp v179, v179, v179 row_ror:1 row_mask:0xf bank_mask:0xf
  s_waitcnt lgkmcnt(4)
  v_max_f32_dpp v180, v116, v116 row_ror:8 row_mask:0xf bank_mask:0xf
  v_max_f32_dpp v181, v117, v117 row_ror:8 row_mask:0xf bank_mask:0xf
  v_max_f32_dpp v182, v118, v118 row_ror:8 row_mask:0xf bank_mask:0xf
  v_max_f32_dpp v183, v119, v119 row_ror:8 row_mask:0xf bank_mask:0xf
  v_max_f32_dpp v180, v180, v180 row_ror:4 row_mask:0xf bank_mask:0xf
  v_max_f32_dpp v181, v181, v181 row_ror:4 row_mask:0xf bank_mask:0xf
  v_max_f32_dpp v182, v182, v182 row_ror:4 row_mask:0xf bank_mask:0xf
  v_max_f32_dpp v183, v183, v183 row_ror:4 row_mask:0xf bank_mask:0xf
  v_max_f32_dpp v180, v180, v180 row_ror:2 row_mask:0xf bank_mask:0xf
  v_max_f32_dpp v181, v181, v181 row_ror:2 row_mask:0xf bank_mask:0xf
  v_max_f32_dpp v182, v182, v182 row_ror:2 row_mask:0xf bank_mask:0xf
  v_max_f32_dpp v183, v183, v183 row_ror:2 row_mask:0xf bank_mask:0xf
  v_max_f32_dpp v180, v180, v180 row_ror:1 row_mask:0xf bank_mask:0xf
  v_max_f32_dpp v181, v181, v181 row_ror:1 row_mask:0xf bank_mask:0xf
  v_max_f32_dpp v182, v182, v182 row_ror:1 row_mask:0xf bank_mask:0xf
  v_max_f32_dpp v183, v183, v183 row_ror:1 row_mask:0xf bank_mask:0xf
  s_waitcnt lgkmcnt(3)
  v_max_f32_dpp v184, v120, v120 row_ror:8 row_mask:0xf bank_mask:0xf
  v_max_f32_dpp v185, v121, v121 row_ror:8 row_mask:0xf bank_mask:0xf
  v_max_f32_dpp v186, v122, v122 row_ror:8 row_mask:0xf bank_mask:0xf
  v_max_f32_dpp v187, v123, v123 row_ror:8 row_mask:0xf bank_mask:0xf
  v_max_f32_dpp v184, v184, v184 row_ror:4 row_mask:0xf bank_mask:0xf
  v_max_f32_dpp v185, v185, v185 row_ror:4 row_mask:0xf bank_mask:0xf
  v_max_f32_dpp v186, v186, v186 row_ror:4 row_mask:0xf bank_mask:0xf
  v_max_f32_dpp v187, v187, v187 row_ror:4 row_mask:0xf bank_mask:0xf
  v_max_f32_dpp v184, v184, v184 row_ror:2 row_mask:0xf bank_mask:0xf
  v_max_f32_dpp v185, v185, v185 row_ror:2 row_mask:0xf bank_mask:0xf
  v_max_f32_dpp v186, v186, v186 row_ror:2 row_mask:0xf bank_mask:0xf
  v_max_f32_dpp v187, v187, v187 row_ror:2 row_mask:0xf bank_mask:0xf
  v_max_f32_dpp v184, v184, v184 row_ror:1 row_mask:0xf bank_mask:0xf
  v_max_f32_dpp v185, v185, v185 row_ror:1 row_mask:0xf bank_mask:0xf
  v_max_f32_dpp v186, v186, v186 row_ror:1 row_mask:0xf bank_mask:0xf
  v_max_f32_dpp v187, v187, v187 row_ror:1 row_mask:0xf bank_mask:0xf
  s_waitcnt lgkmcnt(2)
  v_max_f32_dpp v188, v124, v124 row_ror:8 row_mask:0xf bank_mask:0xf
  v_max_f32_dpp v189, v125, v125 row_ror:8 row_mask:0xf bank_mask:0xf
  v_max_f32_dpp v190, v126, v126 row_ror:8 row_mask:0xf bank_mask:0xf
  v_max_f32_dpp v191, v127, v127 row_ror:8 row_mask:0xf bank_mask:0xf
  v_max_f32_dpp v188, v188, v188 row_ror:4 row_mask:0xf bank_mask:0xf
  v_max_f32_dpp v189, v189, v189 row_ror:4 row_mask:0xf bank_mask:0xf
  v_max_f32_dpp v190, v190, v190 row_ror:4 row_mask:0xf bank_mask:0xf
  v_max_f32_dpp v191, v191, v191 row_ror:4 row_mask:0xf bank_mask:0xf
  v_max_f32_dpp v188, v188, v188 row_ror:2 row_mask:0xf bank_mask:0xf
  v_max_f32_dpp v189, v189, v189 row_ror:2 row_mask:0xf bank_mask:0xf
  v_max_f32_dpp v190, v190, v190 row_ror:2 row_mask:0xf bank_mask:0xf
  v_max_f32_dpp v191, v191, v191 row_ror:2 row_mask:0xf bank_mask:0xf
  v_max_f32_dpp v188, v188, v188 row_ror:1 row_mask:0xf bank_mask:0xf
  v_max_f32_dpp v189, v189, v189 row_ror:1 row_mask:0xf bank_mask:0xf
  v_max_f32_dpp v190, v190, v190 row_ror:1 row_mask:0xf bank_mask:0xf
  v_max_f32_dpp v191, v191, v191 row_ror:1 row_mask:0xf bank_mask:0xf
  s_lshl_b32 s33, s27, 8
  v_add_u32 v254, s33, v255
  s_mov_b32 exec_lo, 0x00010001
  s_mov_b32 exec_hi, 0x00010001
  ds_write_b128 v254, v[176:179] offset:0
  ds_write_b128 v254, v[180:183] offset:16
  ds_write_b128 v254, v[184:187] offset:128
  ds_write_b128 v254, v[188:191] offset:144
  s_mov_b64 exec, -1
  ds_read_b32 v216, v251 offset:1024
  s_waitcnt lgkmcnt(0)
  s_barrier
  ds_read2st64_b32 v[220:221], v251 offset0:0 offset1:1
  ds_read2st64_b32 v[222:223], v251 offset0:2 offset1:3
  s_waitcnt lgkmcnt(1)
  v_max_f32 v220, v220, v221
  s_waitcnt lgkmcnt(0)
  v_max3_f32 v220, v220, v222, v223
  v_ceil_f32_e64 v220, v220
  v_add_f32 v220, 0x42200000, v220
  v_max_f32_e64 v220, 0, v220
  v_min_f32 v220, 0x49742400, v220
  v_add_f32_e64 v216, v216, v220
  ds_write_b32 v251, v220 offset:1280
  ds_write_b32 v251, v216 offset:1024
  ds_read_b128 v[176:179], v255 offset:1280
  ds_read_b128 v[180:183], v255 offset:1296
  ds_read_b128 v[184:187], v255 offset:1408
  ds_read_b128 v[188:191], v255 offset:1424
  s_waitcnt lgkmcnt(3)
  v_add_f32 v96, v96, v176
  v_sub_f32 v112, v112, v176
  v_exp_f32 v220, v112
  s_nop 0
  v_mul_f32 v220, v220, v247
  v_add_f32 v97, v97, v177
  v_sub_f32 v113, v113, v177
  v_exp_f32 v221, v113
  s_nop 0
  v_mul_f32 v221, v221, v247
  v_add_f32 v98, v98, v178
  v_sub_f32 v114, v114, v178
  v_exp_f32 v222, v114
  s_nop 0
  v_mul_f32 v222, v222, v247
  v_add_f32 v99, v99, v179
  v_sub_f32 v115, v115, v179
  v_exp_f32 v223, v115
  s_nop 0
  v_mul_f32_e64 v223, v223, v247
  v_cvt_pk_bf16_f32 v224, v220, v221
  v_cvt_pk_bf16_f32 v225, v222, v223
  s_waitcnt lgkmcnt(2)
  v_add_f32 v100, v100, v180
  v_sub_f32 v116, v116, v180
  v_exp_f32 v220, v116
  s_nop 0
  v_mul_f32 v220, v220, v247
  v_add_f32 v101, v101, v181
  v_sub_f32 v117, v117, v181
  v_exp_f32 v221, v117
  s_nop 0
  v_mul_f32 v221, v221, v247
  v_add_f32 v102, v102, v182
  v_sub_f32 v118, v118, v182
  v_exp_f32 v222, v118
  s_nop 0
  v_mul_f32 v222, v222, v247
  v_add_f32 v103, v103, v183
  v_sub_f32 v119, v119, v183
  v_exp_f32 v223, v119
  s_nop 0
  v_mul_f32_e64 v223, v223, v247
  v_cvt_pk_bf16_f32 v226, v220, v221
  v_cvt_pk_bf16_f32 v227, v222, v223
  s_waitcnt lgkmcnt(1)
  v_add_f32 v104, v104, v184
  v_sub_f32 v120, v120, v184
  v_exp_f32 v220, v120
  s_nop 0
  v_mul_f32 v220, v220, v247
  v_add_f32 v105, v105, v185
  v_sub_f32 v121, v121, v185
  v_exp_f32 v221, v121
  s_nop 0
  v_mul_f32 v221, v221, v247
  v_add_f32 v106, v106, v186
  v_sub_f32 v122, v122, v186
  v_exp_f32 v222, v122
  s_nop 0
  v_mul_f32 v222, v222, v247
  v_add_f32 v107, v107, v187
  v_sub_f32 v123, v123, v187
  v_exp_f32 v223, v123
  s_nop 0
  v_mul_f32_e64 v223, v223, v247
  v_cvt_pk_bf16_f32 v228, v220, v221
  v_cvt_pk_bf16_f32 v229, v222, v223
  s_waitcnt lgkmcnt(0)
  v_add_f32 v108, v108, v188
  v_sub_f32 v124, v124, v188
  v_exp_f32 v220, v124
  s_nop 0
  v_mul_f32 v220, v220, v247
  v_add_f32 v109, v109, v189
  v_sub_f32 v125, v125, v189
  v_exp_f32 v221, v125
  s_nop 0
  v_mul_f32 v221, v221, v247
  v_add_f32 v110, v110, v190
  v_sub_f32 v126, v126, v190
  v_exp_f32 v222, v126
  s_nop 0
  v_mul_f32 v222, v222, v247
  v_add_f32 v111, v111, v191
  v_sub_f32 v127, v127, v191
  v_exp_f32 v223, v127
  s_nop 0
  v_mul_f32_e64 v223, v223, v247
  v_cvt_pk_bf16_f32 v230, v220, v221
  v_cvt_pk_bf16_f32 v231, v222, v223
  ds_write_b128 v242, v[224:227] offset:0
  ds_write_b128 v243, v[228:231] offset:0
  s_waitcnt lgkmcnt(0)
  s_barrier
  s_mov_b32 s44, -1
  s_mov_b32 s3, 0
  s_branch .Lstop
.Lcode1:
)ASM" ::"{s[0:1]}"(__builtin_amdgcn_kernarg_segment_ptr()), "{s2}"(blockIdx.x), "{v0}"(threadIdx.x) : "v1", "v255", "s3", "s99", "vcc", "scc", "memory");
}

extern "C" void kernel_launch(void* const* d_in, const int* in_sizes, int n_in, void* d_out, int out_size, void* d_ws, size_t ws_size,
                              hipStream_t stream) {
  (void)hipFuncSetAttribute((const void*)m2f_main, hipFuncAttributeMaxDynamicSharedMemorySize, 160256);
  m2f_main<<<256, 512, 160256, stream>>>(d_in[0], d_in[1], d_in[2], d_in[3], d_in[4], d_in[5], d_in[6], d_in[7], d_in[8], d_out, (char*)d_ws + WS_BASE(ws_size));
}
```
